```python
import torch

def _softmax(x, axis=-1):
    return torch.softmax(x, dim=axis)
def _gelu(x):
    return torch.nn.functional.gelu(x, approximate="tanh")
def _silu(x):
    return torch.nn.functional.silu(x)
def _sigmoid(x):
    return torch.sigmoid(x)
def _layernorm(x, axis=-1, eps=1e-5):
    m = x.mean(dim=axis, keepdim=True)
    v = x.var(dim=axis, keepdim=True, unbiased=False)
    return (x - m) / torch.sqrt(v + eps)
def _scatter_add(tgt, idx, src, axis=0):
    return tgt.clone().index_add_(axis, idx, src)
def _segment_sum(src, seg, n):
    out = torch.zeros((n,) + src.shape[1:], dtype=src.dtype)
    return out.index_add_(0, seg, src)
def _pad(x, pw):
    flat = tuple(v for pair in reversed(pw) for v in pair)
    return torch.nn.functional.pad(x, flat)

kernel_name = 'parallel_sum__ssm_conv_s4d__moe_arch_mod__prompt'

def setup_inputs():

    _w = (lambda a: (lambda t: t.to(torch.bfloat16) if t.is_floating_point() else t)(torch.as_tensor(a)))
    def _A_setup():
        Bsz, L, D, N = 1, 2048, 1024, 128
        return {
            "u": _w((lambda *s: torch.randn(*s, dtype=torch.bfloat16) * 0.05)(Bsz, L, D)),
            "log_A_real": _w((lambda *s: torch.randn(*s, dtype=torch.bfloat16) * 3.2)(D, N) * 0.5),
            "A_imag": _w((lambda *s: torch.randn(*s, dtype=torch.bfloat16) * 0.2)(D, N) * 3.14),
            "C_real": _w((lambda *s: torch.randn(*s, dtype=torch.bfloat16) * 3.2)(D, N) / N ** 0.5),
            "C_imag": _w((lambda *s: torch.randn(*s, dtype=torch.bfloat16) * 3.2)(D, N) / N ** 0.5),
            "log_dt": _w((lambda *s: torch.randn(*s, dtype=torch.bfloat16) * 0.2)(D) * 0.5 - 4.0),
            "D_skip": _w((lambda *s: torch.randn(*s, dtype=torch.bfloat16) * 0.4)(D)),
        }
    _A_in = _A_setup()

    def _B_setup():
        B, S, D, F, C = 1, 2048, 1024, 3584, 1
        return {
            "x": _w((lambda *s: torch.randn(*s, dtype=torch.bfloat16) * 0.05)(B, S, D)),
            "w_r": _w((lambda *s: torch.randn(*s, dtype=torch.bfloat16) * 0.05)(D)),
            "w_gate": _w((lambda *s: torch.randn(*s, dtype=torch.bfloat16) * 0.05)(D, F)),
            "w_up": _w((lambda *s: torch.randn(*s, dtype=torch.bfloat16) * 0.05)(D, F)),
            "w_down": _w((lambda *s: torch.randn(*s, dtype=torch.bfloat16) * 0.05)(F, D)),
        }
    _B_in = _B_setup()

    return {
        "A_u": _A_in["u"],
        "A_log_A_real": _A_in["log_A_real"],
        "A_A_imag": _A_in["A_imag"],
        "A_C_real": _A_in["C_real"],
        "A_C_imag": _A_in["C_imag"],
        "A_log_dt": _A_in["log_dt"],
        "A_D_skip": _A_in["D_skip"],
        "B_x": _B_in["x"],
        "B_w_r": _B_in["w_r"],
        "B_w_gate": _B_in["w_gate"],
        "B_w_up": _B_in["w_up"],
        "B_w_down": _B_in["w_down"],
    }

def reference(A_u, A_log_A_real, A_A_imag, A_C_real, A_C_imag, A_log_dt, A_D_skip, B_x, B_w_r, B_w_gate, B_w_up, B_w_down):
    def _A_ref(u, log_A_real, A_imag, C_real, C_imag, log_dt, D_skip):
        L = u.shape[1]
        dt = torch.exp(log_dt)
        A = -torch.exp(log_A_real) + 1j * A_imag
        C = (C_real + 1j * C_imag) * dt[:, None]
        pos = (lambda x, dt: x.to(dt))(torch.arange(L), log_dt.dtype)
        P = torch.exp((A * dt[:, None])[:, :, None] * pos[None, None, :])
        K = 2.0 * torch.einsum("dn,dnl->dl", C, P).real
        L2 = 2 * L
        u_t = (lambda x, *d: x.permute(*d))(u, 0, 2, 1)
        Kf = (lambda x, *a, **k: torch.fft.rfft(x.float(), *a, **k))(K, L2)
        Uf = (lambda x, *a, **k: torch.fft.rfft(x.float(), *a, **k))(u_t, L2)
        y = (lambda x, *a, **k: torch.fft.irfft(x, *a, **k).to(torch.bfloat16))(Uf * Kf[None, :, :], L2)[:, :, :L]
        y = (lambda x, *d: x.permute(*d))(y, 0, 2, 1)
        return y + u * D_skip[None, None, :]

    def _B_ref(x, w_r, w_gate, w_up, w_down):
        B, S, D = x.shape
        C = 1
        r = x @ w_r
        idx = torch.argsort(-r, axis=-1)[:, :C]
        idx_e = (lambda x, n, axis: torch.repeat_interleave(x, n, dim=axis))(idx.reshape(B, C, 1), D, axis=2)
        x_sel = (lambda x, i, axis: torch.take_along_dim(x, i, dim=axis))(x, idx_e, axis=1)
        g = _silu(x_sel @ w_gate)
        u = x_sel @ w_up
        y = (g * u) @ w_down
        r_sel = _sigmoid((lambda x, i, axis: torch.take_along_dim(x, i, dim=axis))(r, idx, axis=1))
        y = y * r_sel.reshape(B, C, 1)
        b_off = (torch.arange(B) * S).reshape(B, 1)
        flat_idx = (idx + b_off).reshape(B * C)
        zero = (x * 0.0).reshape(B * S, D)
        upd = _scatter_add(zero, flat_idx, y.reshape(B * C, D), axis=0)
        return x + upd.reshape(B, S, D)

    x = A_u
    xn = _layernorm(x, axis=-1)
    return x + _A_ref(xn, A_log_A_real, A_A_imag, A_C_real, A_C_imag, A_log_dt, A_D_skip) + _B_ref(xn, B_w_r, B_w_gate, B_w_up, B_w_down)
```

```cpp
#include <hip/hip_runtime.h>
#include <hip/hip_bf16.h>
#include <stdint.h>

constexpr int L = 2048, D = 1024, N = 128, F = 3584;
typedef unsigned short u16;
__device__ __forceinline__ float bf2f(u16 v) { return __uint_as_float(((unsigned)v) << 16); }
__device__ __forceinline__ u16 f2bf(float f) { __hip_bfloat16 b = __float2bfloat16(f); return *(u16*)&b; }
__device__ __forceinline__ float rbf(float f) { return bf2f(f2bf(f)); }

__device__ float block_sum(float v, float* s) {
  for (int o = 32; o > 0; o >>= 1) v += __shfl_xor(v, o, 64);
  __syncthreads();
  if ((threadIdx.x & 63) == 0) s[threadIdx.x >> 6] = v;
  __syncthreads();
  float t = 0;
  for (int i = 0; i < (int)(blockDim.x >> 6); ++i) t += s[i];
  return t;
}

__global__ void k_ln(const u16* x, const u16* wr, u16* xn, float* r) {
  __shared__ float s[8];
  int t = blockIdx.x;
  float v[4];
  for (int i = 0; i < 4; ++i) v[i] = bf2f(x[t * D + threadIdx.x * 4 + i]);
  float mean = block_sum(v[0] + v[1] + v[2] + v[3], s) / D;
  float q = 0;
  for (int i = 0; i < 4; ++i) q += (v[i] - mean) * (v[i] - mean);
  float var = block_sum(q, s) / D;
  float m = rbf(mean), vv = rbf(var);
  float sd = rbf(sqrtf(rbf(vv + 1e-5f)));
  float rr = 0;
  for (int i = 0; i < 4; ++i) {
    float z = rbf(rbf(v[i] - m) / sd);
    xn[t * D + threadIdx.x * 4 + i] = f2bf(z);
    rr += z * bf2f(wr[threadIdx.x * 4 + i]);
  }
  rr = block_sum(rr, s);
  if (threadIdx.x == 0) r[t] = rbf(rr);
}

__global__ void k_kgen(const u16* lar, const u16* aim, const u16* cr, const u16* ci, const u16* ldt, float* K) {
  int d = blockIdx.x;
  __shared__ float zr[N], zi[N], c_r[N], c_i[N];
  float dt = rbf(expf(bf2f(ldt[d])));
  if (threadIdx.x < N) {
    int n = threadIdx.x;
    zr[n] = -rbf(expf(bf2f(lar[d * N + n]))) * dt;
    zi[n] = bf2f(aim[d * N + n]) * dt;
    c_r[n] = bf2f(cr[d * N + n]) * dt;
    c_i[n] = bf2f(ci[d * N + n]) * dt;
  }
  __syncthreads();
  for (int l = threadIdx.x; l < L; l += blockDim.x) {
    float pos = rbf((float)l);
    float acc = 0;
    for (int n = 0; n < N; ++n) {
      float e = expf(zr[n] * pos);
      float s, c;
      sincosf(zi[n] * pos, &s, &c);
      acc += c_r[n] * e * c - c_i[n] * e * s;
    }
    K[d * L + l] = 2.f * acc;
  }
}

__global__ void k_conv(const u16* x, const u16* xn, const float* K, const u16* dskip, u16* out) {
  int d = blockIdx.x;
  __shared__ float sk[L], su[L];
  for (int i = threadIdx.x; i < L; i += blockDim.x) { sk[i] = K[d * L + i]; su[i] = bf2f(xn[i * D + d]); }
  __syncthreads();
  float ds = bf2f(dskip[d]);
  for (int t = threadIdx.x; t < L; t += blockDim.x) {
    float acc = 0;
    for (int l = 0; l <= t; ++l) acc += sk[l] * su[t - l];
    float y = rbf(acc);
    float a = rbf(y + rbf(su[t] * ds));
    float o = rbf(bf2f(x[t * D + d]) + a);
    out[t * D + d] = f2bf(o);
  }
}

__global__ void k_argmax(const float* r, int* idx) {
  __shared__ float sv[256]; __shared__ int si[256];
  float bv = -INFINITY; int bi = 0;
  for (int i = threadIdx.x; i < L; i += 256) { if (r[i] > bv) { bv = r[i]; bi = i; } }
  sv[threadIdx.x] = bv; si[threadIdx.x] = bi;
  __syncthreads();
  if (threadIdx.x == 0) {
    for (int i = 1; i < 256; ++i) if (sv[i] > bv || (sv[i] == bv && si[i] < bi)) { bv = sv[i]; bi = si[i]; }
    *idx = bi;
  }
}

__global__ void k_gateup(const u16* xn, const int* idx, const u16* wg, const u16* wu, float* h) {
  int f = blockIdx.x * blockDim.x + threadIdx.x;
  if (f >= F) return;
  const u16* xs = xn + (*idx) * D;
  float g = 0, u = 0;
  for (int k = 0; k < D; ++k) { float xv = bf2f(xs[k]); g += xv * bf2f(wg[k * F + f]); u += xv * bf2f(wu[k * F + f]); }
  g = rbf(g); u = rbf(u);
  g = rbf(g / (1.f + expf(-g)));
  h[f] = rbf(g * u);
}

__global__ void k_final(const u16* xn, const int* idx, const float* r, const float* h, const u16* wd, u16* out) {
  int t = blockIdx.x;
  int id = *idx;
  for (int d = threadIdx.x; d < D; d += blockDim.x) {
    float upd = 0;
    if (t == id) {
      float y = 0;
      for (int f = 0; f < F; ++f) y += h[f] * bf2f(wd[f * D + d]);
      y = rbf(y);
      float rs = rbf(1.f / (1.f + expf(-r[id])));
      upd = rbf(y * rs);
    }
    float b = rbf(bf2f(xn[t * D + d]) + upd);
    out[t * D + d] = f2bf(bf2f(out[t * D + d]) + b);
  }
}

extern "C" void kernel_launch(void* const* d_in, const int* in_sizes, int n_in,
                              void* d_out, int out_size, void* d_ws, size_t ws_size,
                              hipStream_t stream) {
  const u16* x = (const u16*)d_in[0];
  const u16* lar = (const u16*)d_in[1];
  const u16* aim = (const u16*)d_in[2];
  const u16* cr = (const u16*)d_in[3];
  const u16* ci = (const u16*)d_in[4];
  const u16* ldt = (const u16*)d_in[5];
  const u16* dsk = (const u16*)d_in[6];
  const u16* wr = (const u16*)d_in[8];
  const u16* wg = (const u16*)d_in[9];
  const u16* wu = (const u16*)d_in[10];
  const u16* wd = (const u16*)d_in[11];
  u16* out = (u16*)d_out;
  char* ws = (char*)d_ws;
  u16* xn = (u16*)ws;
  float* K = (float*)(ws + (4 << 20));
  float* r = (float*)(ws + (12 << 20));
  int* idx = (int*)(ws + (12 << 20) + 16384);
  float* h = (float*)(ws + (12 << 20) + 32768);
  k_ln<<<L, 256, 0, stream>>>(x, wr, xn, r);
  k_kgen<<<D, 256, 0, stream>>>(lar, aim, cr, ci, ldt, K);
  k_conv<<<D, 256, 0, stream>>>(x, xn, K, dsk, out);
  k_argmax<<<1, 256, 0, stream>>>(r, idx);
  k_gateup<<<F / 64, 64, 0, stream>>>(xn, idx, wg, wu, h);
  k_final<<<L, 256, 0, stream>>>(xn, idx, r, h, wd, out);
}
```

```cpp
#include <hip/hip_runtime.h>
#include <stdint.h>

typedef unsigned short u16;
typedef unsigned int u32;
typedef __attribute__((ext_vector_type(2))) u32 u32x2;
typedef __attribute__((ext_vector_type(4))) u32 u32x4;
typedef __attribute__((ext_vector_type(2))) float f32x2;
typedef __attribute__((ext_vector_type(4))) float f32x4;
typedef __attribute__((ext_vector_type(16))) float f32x16;
typedef __attribute__((ext_vector_type(8))) __bf16 bf16x8;
typedef __attribute__((ext_vector_type(2))) __bf16 bf16x2;
typedef __attribute__((ext_vector_type(8))) _Float16 f16x8;
typedef __attribute__((ext_vector_type(2))) _Float16 f16x2;
typedef u32x2 __attribute__((aligned(2))) u32x2_u;

constexpr int L = 2048, D = 1024, NM = 128, F = 3584;

#define DEVI __device__ __forceinline__
DEVI float lo16(u32 u) { return __uint_as_float(u << 16); }
DEVI float hi16(u32 u) { return __uint_as_float(u & 0xffff0000u); }
DEVI float bf2f(u16 v) { return __uint_as_float(((u32)v) << 16); }
DEVI u32 pkbf(float a, float b) { f32x2 v = {a, b}; return __builtin_bit_cast(u32, __builtin_convertvector(v, bf16x2)); }
DEVI u32 pkh(float a, float b) { f32x2 v = {a, b}; return __builtin_bit_cast(u32, __builtin_convertvector(v, f16x2)); }
DEVI float rbf(float a) { return lo16(pkbf(a, a)); }

template <int CTRL> DEVI float dppf(float v) {
  return __uint_as_float(__builtin_amdgcn_update_dpp(0u, __float_as_uint(v), CTRL, 0xf, 0xf, true));
}
template <int CTRL> DEVI u32 dppu(u32 v) { return __builtin_amdgcn_update_dpp(0u, v, CTRL, 0xf, 0xf, true); }
constexpr int DPP_X1 = 0xB1, DPP_X2 = 0x4E, DPP_HMIR = 0x141, DPP_MIR = 0x140, DPP_ROR4 = 0x124, DPP_ROR8 = 0x128, DPP_WSHR1 = 0x138;
DEVI float row_allsum(float v) {
  v += dppf<DPP_X1>(v); v += dppf<DPP_X2>(v); v += dppf<DPP_HMIR>(v); v += dppf<DPP_MIR>(v); return v;
}
DEVI float sw32(float a, float b) {
  auto r = __builtin_amdgcn_permlane32_swap(__float_as_uint(a), __float_as_uint(b), false, false);
  return __uint_as_float(r[0]) + __uint_as_float(r[1]);
}
DEVI float sw16(float a, float b) {
  auto r = __builtin_amdgcn_permlane16_swap(__float_as_uint(a), __float_as_uint(b), false, false);
  return __uint_as_float(r[0]) + __uint_as_float(r[1]);
}
DEVI u32 umax32(u32 a, u32 b) { return a > b ? a : b; }
DEVI u32 wave_umax(u32 v) {
  v = umax32(v, dppu<DPP_X1>(v)); v = umax32(v, dppu<DPP_X2>(v)); v = umax32(v, dppu<DPP_HMIR>(v)); v = umax32(v, dppu<DPP_MIR>(v));
  u32 a = __builtin_amdgcn_readlane(v, 0), b = __builtin_amdgcn_readlane(v, 16), c = __builtin_amdgcn_readlane(v, 32), d = __builtin_amdgcn_readlane(v, 48);
  return umax32(umax32(a, b), umax32(c, d));
}
DEVI float rdl(float v, int l) { return __uint_as_float(__builtin_amdgcn_readlane(__float_as_uint(v), l)); }

constexpr int I3E = 127, I3O = 223, I2A = 255, I2B = 319, I2C = 383, I2D = 447, LM = 975, IX = 1016;
constexpr int ZIDX = 24 * 32;
constexpr int rnd_bf16_int(int l) {
  if (l < 256) return l;
  int s = 0; while ((l >> s) >= 256) ++s;
  int q = l >> s, rem = l & ((1 << s) - 1), half = 1 << (s - 1);
  if (rem > half || (rem == half && (q & 1))) ++q;
  return q << s;
}
constexpr int pos_idx(int p) {
  if (p == 0) return 0;
  int row = 0, alpha = 0;
  if (p <= 256) { alpha = ((p - 1) & 7) + 1; row = alpha; }
  else if (p <= 512) { int o = ((p - 258) & 7) >> 1; alpha = 258 + 2 * o; row = 9 + o; }
  else if (p <= 1024) { int i = (p - 516) >> 8; int o = ((p - 516) & 7) >> 2; alpha = 516 + 256 * i + 4 * o; row = 13 + 2 * i + o; }
  else { int i = (p - 1032) >> 8; alpha = 1032 + 256 * i; row = 17 + i; }
  return row * 32 + (p - alpha) / 8;
}
struct Lut { u16 v[1024]; };
constexpr Lut make_lut() {
  Lut t{};
  for (int i = 0; i < 1024; ++i) t.v[i] = ZIDX;
  for (int m = 64; m <= 127; ++m) { t.v[I3E - m] = pos_idx(16 * (m + 1)); t.v[I3O - m] = pos_idx(16 * m + 8); }
  for (int m = 32; m <= 63; ++m) {
    t.v[I2A - m] = pos_idx(16 * (m + 1));
    if (m <= 62) t.v[I2B - m] = pos_idx(16 * (m + 1) + 4);
    t.v[I2C - m] = pos_idx(16 * m + 8);
    t.v[I2D - m] = pos_idx(16 * m + 12);
  }
  for (int l = 0; l <= 517; ++l) t.v[LM - l] = pos_idx(rnd_bf16_int(l));
  t.v[IX] = pos_idx(1024);
  return t;
}
__device__ const Lut g_lut = make_lut();

constexpr size_t WS_XT = 0;
constexpr size_t WS_KT = 4u << 20;
constexpr size_t WS_PART = (6u << 20) + 4096;
constexpr size_t WS_CAND = WS_PART + 224 * 4096;
constexpr size_t WS_STAT = WS_CAND + 256 * 2048;
constexpr size_t WS_KEYS = WS_STAT + 2048 * 8;
constexpr size_t WS_SEL = WS_KEYS + 1024;

struct C2 { float r, i; };
DEVI C2 cmul(C2 a, C2 b) { C2 c; c.r = a.r * b.r - a.i * b.i; c.i = a.r * b.i + a.i * b.r; return c; }
DEVI C2 cexp2(float lg, float rev) {
  float m = __builtin_amdgcn_exp2f(lg);
  C2 c; c.r = m * __builtin_amdgcn_cosf(rev); c.i = m * __builtin_amdgcn_sinf(rev); return c;
}

constexpr int KG_ROW = 272, KG_WAVE = 32 * KG_ROW;

__global__ __launch_bounds__(512) void k1(const u16* __restrict__ x, const u16* __restrict__ wr,
                                          const u16* __restrict__ lar, const u16* __restrict__ aim,
                                          const u16* __restrict__ cre, const u16* __restrict__ cim,
                                          const u16* __restrict__ ldt, char* __restrict__ ws) {
  __shared__ __attribute__((aligned(16))) char smem[8 * KG_WAVE];
  const int tid = threadIdx.x, lane = tid & 63, wv = __builtin_amdgcn_readfirstlane(tid >> 6);
  if (blockIdx.x < 256) {
    const int blk = blockIdx.x;
    const u32* xp = (const u32*)(x + blk * 8 * D) + tid;
    u32 v[8];
#pragma unroll
    for (int r = 0; r < 8; ++r) v[r] = xp[r * 512];
    const float pv = bf2f(x[(blk * 8 + (lane & 7)) * D]);
    const u32 w = ((const u32*)wr)[tid];
    float* part = (float*)smem;
    float* part2 = part + 128;
    float s[16];
#pragma unroll
    for (int r = 0; r < 8; ++r) {
      float p = rdl(pv, r);
      float a = lo16(v[r]) - p, b = hi16(v[r]) - p;
      s[r] = a + b; s[8 + r] = a * a + b * b;
    }
#pragma unroll
    for (int i = 0; i < 8; ++i) s[i] = sw32(s[i], s[i + 8]);
#pragma unroll
    for (int i = 0; i < 4; ++i) s[i] = row_allsum(sw16(s[i], s[i + 4]));
    if ((lane & 15) == 0) *(f32x4*)(part + wv * 16 + (lane >> 4) * 4) = f32x4{s[0], s[1], s[2], s[3]};
    __syncthreads();
    float t1 = part[(lane >> 3) * 16 + (lane & 7)], t2 = part[(lane >> 3) * 16 + 8 + (lane & 7)];
    t1 += dppf<DPP_ROR8>(t1); t2 += dppf<DPP_ROR8>(t2);
    t1 = sw16(t1, t1); t2 = sw16(t2, t2);
    t1 = sw32(t1, t1); t2 = sw32(t2, t2);
    t1 *= (1.f / D);
    float var = fmaxf(t2 * (1.f / D) - t1 * t1, 0.f);
    float m = rbf(pv + t1);
    float sd = rbf(__builtin_amdgcn_sqrtf(rbf(rbf(var) + 1e-5f)));
    float rs = __builtin_amdgcn_rcpf(sd);
    if (tid < 8) ((f32x2*)(ws + WS_STAT))[blk * 8 + tid] = f32x2{m, rs};
    u32 c0[4], c1[4];
    float dt[8];
    const float w0 = lo16(w), w1 = hi16(w);
#pragma unroll
    for (int k = 0; k < 4; ++k) {
      float ma = rdl(m, 2 * k), mb = rdl(m, 2 * k + 1), ra = rdl(rs, 2 * k), rb = rdl(rs, 2 * k + 1);
      u32 da = pkbf(lo16(v[2 * k]) - ma, hi16(v[2 * k]) - ma);
      u32 db = pkbf(lo16(v[2 * k + 1]) - mb, hi16(v[2 * k + 1]) - mb);
      c0[k] = pkbf(lo16(da) * ra, lo16(db) * rb);
      c1[k] = pkbf(hi16(da) * ra, hi16(db) * rb);
      dt[2 * k] = lo16(c0[k]) * w0 + lo16(c1[k]) * w1;
      dt[2 * k + 1] = hi16(c0[k]) * w0 + hi16(c1[k]) * w1;
    }
    u32x4* xt = (u32x4*)(ws + WS_XT) + blk * 1024;
    xt[tid] = u32x4{c0[0], c0[1], c0[2], c0[3]};
    xt[512 + tid] = u32x4{c1[0], c1[1], c1[2], c1[3]};
#pragma unroll
    for (int i = 0; i < 4; ++i) dt[i] = sw32(dt[i], dt[i + 4]);
#pragma unroll
    for (int i = 0; i < 2; ++i) dt[i] = row_allsum(sw16(dt[i], dt[i + 2]));
    if ((lane & 15) == 0) *(f32x2*)(part2 + wv * 8 + (lane >> 4) * 2) = f32x2{dt[0], dt[1]};
    __syncthreads();
    float rr = part2[lane];
    rr += dppf<DPP_ROR8>(rr); rr = sw16(rr, rr); rr = sw32(rr, rr);
    u32 b = pkbf(rr, rr) & 0xffffu;
    b = (b & 0x8000u) ? (~b & 0xffffu) : (b | 0x8000u);
    u32 key = (b << 16) | (u32)(2047 - (blk * 8 + (lane & 7)));
    key = wave_umax(key);
    if (tid == 0) ((u32*)(ws + WS_KEYS))[blk] = key;
    const int rbest = (2047 - (int)(key & 0xffffu)) & 7;
    u32 a0 = c0[0], a1 = c1[0];
#pragma unroll
    for (int k = 1; k < 4; ++k) if ((rbest >> 1) == k) { a0 = c0[k]; a1 = c1[k]; }
    u32 o = (rbest & 1) ? ((a0 >> 16) | (a1 & 0xffff0000u)) : ((a0 & 0xffffu) | (a1 << 16));
    ((u32*)(ws + WS_CAND))[blk * 512 + tid] = o;
  } else {
    const int kb = blockIdx.x - 256;
    const int pos = kb * 4 + (wv >> 1);
    const int d = 2 * (pos & 511) + (pos >> 9);
    const int n = d * NM + (wv & 1) * 64 + lane;
    const float f_lar = bf2f(lar[n]), f_aim = bf2f(aim[n]), f_cr = bf2f(cre[n]), f_ci = bf2f(cim[n]);
    const float f_ldt = bf2f(ldt[d]);
    const u32x4 lut = *(const u32x4*)(g_lut.v + (tid & 127) * 8);
    const float dtv = rbf(expf(f_ldt));
    const float zr = -rbf(expf(f_lar)) * dtv;
    const float zi = f_aim * dtv;
    const float lg = zr * 1.44269504088896341f;
    const float c_hi = 0.15915494309189535f, c_lo = 6.4206382e-9f;
    float p = zi * c_hi;
    float tl = __builtin_fmaf(zi, c_hi, -p) + zi * c_lo;
    float th = p - __builtin_rintf(p);
    C2 e1 = cexp2(lg, th + tl);
    C2 e8 = cexp2(lg * 8.f, __builtin_amdgcn_fractf(th * 8.f) + tl * 8.f);
    C2 e256 = cexp2(lg * 256.f, __builtin_amdgcn_fractf(th * 256.f) + tl * 256.f);
    float cr2 = 2.f * f_cr * dtv, ci2 = 2.f * f_ci * dtv;
    u32 mx = umax32(__float_as_uint(cr2) & 0x7fffffffu, __float_as_uint(ci2) & 0x7fffffffu);
    mx = wave_umax(mx);
    int ex = (int)(mx >> 23); ex = ex < 1 ? 1 : (ex > 253 ? 253 : ex);
    const float sinv = __uint_as_float((u32)(254 - ex) << 23);
    char* base = smem + wv * KG_WAVE;
    u32* wp = (u32*)(base + lane * 4);
#define WROW(rw, cc) wp[(rw) * (KG_ROW / 4)] = pkh((cc).r, (cc).i)
    wp[0] = 0x00003c00u;
    C2 e2 = cmul(e1, e1), e3 = cmul(e2, e1), e4 = cmul(e2, e2), e5 = cmul(e4, e1), e6 = cmul(e4, e2), e7 = cmul(e4, e3);
    WROW(1, e1); WROW(2, e2); WROW(3, e3); WROW(4, e4); WROW(5, e5); WROW(6, e6); WROW(7, e7); WROW(8, e8);
    { C2 t = cmul(e2, e256); WROW(9, t); t = cmul(e6, e256); WROW(11, t); }
    { C2 t = cmul(e4, e256); WROW(10, t); t = cmul(t, e256); WROW(13, t); t = cmul(t, e256); WROW(15, t); }
    { C2 t = cmul(e8, e256); WROW(12, t); t = cmul(t, e256); WROW(14, t); t = cmul(t, e256); WROW(16, t);
      t = cmul(t, e256); WROW(17, t); t = cmul(t, e256); WROW(18, t); t = cmul(t, e256); WROW(19, t); t = cmul(t, e256); WROW(20, t); }
    const char* rp = base + (lane & 31) * KG_ROW + (lane >> 5) * 16;
    f16x8 af[8];
    asm volatile("" ::: "memory");
#pragma unroll
    for (int s = 0; s < 8; ++s) af[s] = *(const f16x8*)(rp + s * 32);
    asm volatile("" ::: "memory");
    C2 c8; c8.r = e8.r; c8.i = -e8.i;
    C2 c16 = cmul(c8, c8);
    C2 g0, g1; g0.r = cr2 * sinv; g0.i = -ci2 * sinv;
    g1 = cmul(g0, c8);
#pragma unroll
    for (int k = 0; k < 16; ++k) {
      WROW(2 * k, g0); WROW(2 * k + 1, g1);
      if (k < 15) { g0 = cmul(g0, c16); g1 = cmul(g1, c16); }
    }
    asm volatile("" ::: "memory");
    f32x16 acc = {};
#pragma unroll
    for (int s = 0; s < 8; ++s) {
      f16x8 bfr = *(const f16x8*)(rp + s * 32);
      acc = __builtin_amdgcn_mfma_f32_32x32x16_f16(af[s], bfr, acc, 0, 0, 0);
    }
    asm volatile("" ::: "memory");
    float* kp = (float*)base;
#pragma unroll
    for (int r = 0; r < 12; ++r) kp[((r & 3) + 8 * (r >> 2) + 4 * (lane >> 5)) * 32 + (lane & 31)] = __builtin_amdgcn_ldexpf(acc[r], ex - 127);
    if (lane == 0) kp[ZIDX] = 0.f;
    __syncthreads();
    const float* k0 = (const float*)(smem + (wv & ~1) * KG_WAVE);
    const float* k1p = k0 + KG_WAVE / 4;
    u32 o[4];
#pragma unroll
    for (int j = 0; j < 4; ++j) {
      u32 ia = lut[j] & 0xffffu, ib = lut[j] >> 16;
      o[j] = pkbf(k0[ia] + k1p[ia], k0[ib] + k1p[ib]);
    }
    ((u32x4*)(ws + WS_KT))[kb * 512 + tid] = u32x4{o[0], o[1], o[2], o[3]};
  }
}

constexpr int U_STRIDE = 4640;
constexpr int KT0 = 4 * U_STRIDE;
constexpr int YW0 = KT0 + 4 * 8192;
constexpr int YW_STRIDE = 6592;
constexpr int MISC0 = YW0 + 4 * YW_STRIDE;
constexpr int K2_LDS = MISC0 + 1024;

DEVI u32x4 make_box(int ce, int w, int rho, int kg, u32 val) {
  int base = 8 * kg - 16 - rho - ce;
  int sh = 22 + base; sh = sh < 0 ? 0 : (sh > 31 ? 31 : sh);
  u32 bits = (((1u << w) - 1u) << 22) >> sh;
  u32x4 o;
#pragma unroll
  for (int p = 0; p < 4; ++p) o[p] = ((((bits >> (2 * p)) & 3u) * 0x8001u) & 0x10001u) * val;
  return o;
}
DEVI bf16x8 asbf(u32x4 v) { return __builtin_bit_cast(bf16x8, v); }
DEVI bf16x8 ld2x64(const char* p, int off2) {
  u32x2 a = *(const u32x2*)p, b = *(const u32x2*)(p + off2);
  return asbf(u32x4{a[0], a[1], b[0], b[1]});
}
DEVI f32x4 mf16(bf16x8 a, bf16x8 b, f32x4 c) { return __builtin_amdgcn_mfma_f32_16x16x32_bf16(a, b, c, 0, 0, 0); }
DEVI bf16x8 packx(f32x4 a, f32x4 b) { return asbf(u32x4{pkbf(a[0], a[1]), pkbf(a[2], a[3]), pkbf(b[0], b[1]), pkbf(b[2], b[3])}); }
DEVI float dot2(u32 a, u32 b, float c) {
  return __builtin_amdgcn_fdot2_f32_bf16(__builtin_bit_cast(bf16x2, a), __builtin_bit_cast(bf16x2, b), c, false);
}
DEVI void lds_wait_eq(volatile u32* p, u32 v) {
  for (int i = 0; i < (1 << 22) && __builtin_amdgcn_readfirstlane(*p) < v; ++i) __builtin_amdgcn_s_sleep(1);
  asm volatile("" ::: "memory");
}

__global__ __launch_bounds__(768) void k2(const u16* __restrict__ wg, const u16* __restrict__ wu,
                                          const u16* __restrict__ wd, char* __restrict__ ws) {
  __shared__ __attribute__((aligned(16))) char smem[K2_LDS];
  const int tid = threadIdx.x, lane = tid & 63, wv = __builtin_amdgcn_readfirstlane(tid >> 6);
  const int bx = blockIdx.x & 7, bk = blockIdx.x >> 3;
  volatile u32* flags = (volatile u32*)(smem + MISC0);
  if (wv < 8) {
    const int B = bx * 32 + bk;
    const int c = wv & 3;
    u32x4* xt = (u32x4*)(ws + WS_XT) + B * 4;
    const int stb = tid >> 2, sc = tid & 3;
    u32x4 u0 = xt[stb * 1024 + sc], u1 = xt[(stb + 128) * 1024 + sc];
    const u16* kt = (const u16*)(ws + WS_KT) + B * 4096;
    u32x2 kv[8];
#pragma unroll
    for (int i = 0; i < 8; ++i) kv[i] = *(const u32x2_u*)(kt + i * 512 + tid);
    const u32 kx = kt[c * 1024 + IX];
    if (tid < 48) *(u32*)(smem + (tid / 12) * U_STRIDE + (tid % 12) * 4) = 0u;
    else if (tid < 64) flags[tid - 48] = 0u;
    else if (tid < 96) *(float*)(smem + YW0 + ((tid - 64) >> 3) * YW_STRIDE + (tid & 7) * 4) = 0.f;
    char* sdst = smem + sc * U_STRIDE + (stb + (stb >> 3) + 3) * 16;
    const char* ub = smem + c * U_STRIDE;
    const char* kb = smem + KT0 + c * 8192;
    if (wv < 4) {
      *(u32x4*)sdst = u0; *(u32x4*)(sdst + 144 * 16) = u1;
#pragma unroll
      for (int i = 0; i < 8; ++i) *(u32x2*)(smem + KT0 + (i * 512 + tid) * 8) = kv[i];
      asm volatile("s_waitcnt lgkmcnt(0)" ::: "memory");
      __builtin_amdgcn_s_barrier();
      asm volatile("" ::: "memory");
      const int a = lane & 31, h = lane >> 5;
      u32x4 Bq[4];
#pragma unroll
      for (int q = 0; q < 4; ++q) Bq[q] = *(const u32x4*)(ub + (9 * a + 2 * q + h + 3) * 16);
      u32 msk = lane == 32 ? 0u : ~0u;
      asm volatile("" : "+v"(msk));
      const char* ap = kb + (LM - 16 * 33 - a + 8 * h) * 8;
      f32x16 acc0 = {}, acc1 = {};
      bf16x8 A[37];
#pragma unroll
      for (int s = 0; s < 37; ++s) {
        const int q = 3 - (s & 3);
        if (s <= 34) {
          A[s] = ld2x64(ap + (33 - (s - 1)) * 128, 32);
          acc1 = __builtin_amdgcn_mfma_f32_32x32x16_bf16(A[s], asbf(Bq[q]), acc1, 0, 0, 0);
        }
        if (s >= 2) acc0 = __builtin_amdgcn_mfma_f32_32x32x16_bf16(A[s - 2], asbf(Bq[q]), acc0, 0, 0, 0);
        if (s + 4 < 37) {
#pragma unroll
          for (int j = 0; j < 4; ++j) Bq[q][j] = dppu<DPP_WSHR1>(Bq[q][j]) & msk;
        }
      }
      lds_wait_eq(flags + c, 1);
      if (a >= 8) {
        const char* yp = smem + YW0 + c * YW_STRIDE + ((a - 8) * 68 + 4 * h) * 4;
#pragma unroll
        for (int g = 0; g < 4; ++g) {
          f32x4 y0 = *(const f32x4*)(yp + g * 32), y1 = *(const f32x4*)(yp + 128 + g * 32);
#pragma unroll
          for (int i = 0; i < 4; ++i) { acc0[4 * g + i] += y0[i]; acc1[4 * g + i] += y1[i]; }
        }
      }
      char* op = smem + c * U_STRIDE + (9 * a + 3) * 16 + 8 * h;
#pragma unroll
      for (int g = 0; g < 4; ++g) {
        *(u32x2*)(op + g * 16) = u32x2{pkbf(acc0[4 * g], acc0[4 * g + 1]), pkbf(acc0[4 * g + 2], acc0[4 * g + 3])};
        *(u32x2*)(op + 64 + g * 16) = u32x2{pkbf(acc1[4 * g], acc1[4 * g + 1]), pkbf(acc1[4 * g + 2], acc1[4 * g + 3])};
      }
      asm volatile("s_waitcnt lgkmcnt(0)" ::: "memory");
      if (lane == 0) __hip_atomic_fetch_add((u32*)(smem + MISC0) + 4, 1u, __ATOMIC_RELAXED, __HIP_MEMORY_SCOPE_WORKGROUP);
    } else {
      const int rho = lane & 15, kg = lane >> 4;
      const u32x4 bxa = make_box(-13, 5, rho, kg, 0x3f80u), bxb = make_box(-16, 3, rho, kg, 0x3f80u);
      const u32x4 bxc = make_box(-5, 5, rho, kg, 0x3f80u), bxd = make_box(-8, 3, rho, kg, 0x3f80u);
      const u32x4 bxe = make_box(-15, 9, rho, kg, 0x3f80u), bxo = make_box(-6, 7, rho, kg, 0x3f80u);
      const u32x4 bxx = make_box(-15, 2, rho, kg, kx);
      *(u32x4*)sdst = u0; *(u32x4*)(sdst + 144 * 16) = u1;
#pragma unroll
      for (int i = 0; i < 8; ++i) *(u32x2*)(smem + KT0 + (i * 512 + tid) * 8) = kv[i];
      asm volatile("s_waitcnt lgkmcnt(0)" ::: "memory");
      __builtin_amdgcn_s_barrier();
      asm volatile("" ::: "memory");
      bf16x8 uf[6];
      {
        const int pc = 2 * rho - 2 + kg;
        const char* up = ub + (pc + (pc >> 3) + 3) * 16;
#pragma unroll
        for (int b = 0; b < 6; ++b) uf[b] = asbf(*(const u32x4*)(up + b * 576));
      }
      f32x4 Y[6];
      {
        const int pc = 2 * rho + kg;
        const char* up = ub + (pc + (pc >> 3) + 3) * 16;
        int pm = pc - 32; pm = pm < -2 ? -2 : pm;
        const char* um = ub + (pm + (pm >> 3) + 3) * 16;
        const f32x4 z = {};
        Y[0] = z;
        Y[1] = mf16(asbf(*(const u32x4*)um), asbf(bxx), z);
#pragma unroll
        for (int b = 0; b < 4; ++b) Y[2 + b] = mf16(asbf(*(const u32x4*)(up + b * 576)), asbf(bxx), z);
      }
      const char* kp = kb + (4 * kg - rho) * 8;
      auto r2 = [&](u32x4 box, int I) {
        const f32x4 z = {};
        bf16x8 Bf[3], Af[4];
#pragma unroll
        for (int i = 0; i < 4; ++i) Af[i] = ld2x64(kp + (I - 32 - 16 * i) * 8, 128);
#pragma unroll
        for (int S = 0; S < 3; ++S) Bf[S] = packx(mf16(uf[2 * S], asbf(box), z), mf16(uf[2 * S + 1], asbf(box), z));
#pragma unroll
        for (int S = 0; S < 3; ++S)
#pragma unroll
          for (int i = 0; i < 4; ++i) if (2 * S + i < 6) Y[2 * S + i] = mf16(Af[i], Bf[S], Y[2 * S + i]);
      };
      auto r3 = [&](u32x4 box, int I) {
        const f32x4 z = {};
        bf16x8 Bf[2], Af[4];
#pragma unroll
        for (int i = 0; i < 4; ++i) Af[i] = ld2x64(kp + (I - 64 - 16 * i) * 8, 128);
#pragma unroll
        for (int S = 0; S < 2; ++S) Bf[S] = packx(mf16(uf[2 * S], asbf(box), z), mf16(uf[2 * S + 1], asbf(box), z));
#pragma unroll
        for (int S = 0; S < 2; ++S)
#pragma unroll
          for (int i = 0; i < 4; ++i) if (2 + 2 * S + i < 6) Y[2 + 2 * S + i] = mf16(Af[i], Bf[S], Y[2 + 2 * S + i]);
      };
      r2(bxa, I2A); r2(bxb, I2B); r2(bxc, I2C); r2(bxd, I2D); r3(bxe, I3E); r3(bxo, I3O);
      float* yw = (float*)(smem + YW0 + c * YW_STRIDE);
#pragma unroll
      for (int i = 0; i < 4; ++i) {
        const int u = 64 * kg + 16 * i + rho + 5;
        float* q = yw + u + 4 * (u >> 6);
#pragma unroll
        for (int T = 0; T < 6; ++T) q[T * 272] = Y[T][i];
      }
      asm volatile("s_waitcnt lgkmcnt(0)" ::: "memory");
      if (lane == 0) flags[c] = 1u;
    }
    lds_wait_eq(flags + 4, 4);
    xt[stb * 1024 + sc] = *(const u32x4*)sdst;
    xt[(stb + 128) * 1024 + sc] = *(const u32x4*)(sdst + 144 * 16);
  } else {
    const int mt = tid - 512;
    if (bk >= 28) { __builtin_amdgcn_s_barrier(); return; }
    const int cs = 4 * (8 * (bk >> 2) + bx) + (bk & 3);
    const u32x4 kk = ((const u32x4*)(ws + WS_KEYS))[lane];
    const int rg = mt >> 1, half = mt & 1;
    u32x4 g[8], u[8];
    u32x2 dn[16];
    {
      const u16* pg = wg + (size_t)(8 * rg) * F + 16 * cs + 8 * half;
      const u16* pu = wu + (size_t)(8 * rg) * F + 16 * cs + 8 * half;
#pragma unroll
      for (int i = 0; i < 8; ++i) { g[i] = *(const u32x4*)(pg + i * F); u[i] = *(const u32x4*)(pu + i * F); }
      const u16* pd = wd + (size_t)(16 * cs) * D + 4 * mt;
#pragma unroll
      for (int j = 0; j < 16; ++j) dn[j] = *(const u32x2*)(pd + j * D);
    }
    __builtin_amdgcn_s_barrier();
    const u32 key = wave_umax(umax32(umax32(kk[0], kk[1]), umax32(kk[2], kk[3])));
    const int idx = 2047 - (int)(key & 0xffffu);
    if (blockIdx.x == 0 && mt == 0) *(u32*)(ws + WS_SEL) = key;
    const u32x4 xv = *(const u32x4*)(ws + WS_CAND + (idx >> 3) * 2048 + rg * 16);
    float s[16];
#pragma unroll
    for (int i = 0; i < 16; ++i) s[i] = 0.f;
#pragma unroll
    for (int p = 0; p < 4; ++p) {
#pragma unroll
      for (int j = 0; j < 4; ++j) {
        s[2 * j] = dot2(__builtin_amdgcn_perm(g[2 * p + 1][j], g[2 * p][j], 0x05040100u), xv[p], s[2 * j]);
        s[2 * j + 1] = dot2(__builtin_amdgcn_perm(g[2 * p + 1][j], g[2 * p][j], 0x07060302u), xv[p], s[2 * j + 1]);
        s[8 + 2 * j] = dot2(__builtin_amdgcn_perm(u[2 * p + 1][j], u[2 * p][j], 0x05040100u), xv[p], s[8 + 2 * j]);
        s[8 + 2 * j + 1] = dot2(__builtin_amdgcn_perm(u[2 * p + 1][j], u[2 * p][j], 0x07060302u), xv[p], s[8 + 2 * j + 1]);
      }
    }
#pragma unroll
    for (int i = 0; i < 8; ++i) s[i] = sw32(s[i], s[i + 8]);
#pragma unroll
    for (int i = 0; i < 4; ++i) {
      float t = sw16(s[i], s[i + 4]);
      t += dppf<DPP_X2>(t); t += dppf<DPP_ROR4>(t); t += dppf<DPP_ROR8>(t);
      s[i] = t;
    }
    float* red = (float*)(smem + MISC0 + 64);
    if ((lane & 15) < 2) *(f32x4*)(red + (wv - 8) * 32 + (lane & 1) * 16 + (lane >> 4) * 4) = f32x4{s[0], s[1], s[2], s[3]};
    asm volatile("s_waitcnt lgkmcnt(0)" ::: "memory");
    if (lane == 0) __hip_atomic_fetch_add((u32*)(smem + MISC0) + 5, 1u, __ATOMIC_RELAXED, __HIP_MEMORY_SCOPE_WORKGROUP);
    lds_wait_eq(flags + 5, 4);
    const int f = lane & 15;
    const float* rp = red + (f >> 3) * 16 + (f & 7);
    float gs = rbf((rp[0] + rp[32]) + (rp[64] + rp[96]));
    float us = rbf((rp[8] + rp[40]) + (rp[72] + rp[104]));
    float sg = rbf(gs / (1.f + expf(-gs)));
    u32 hb = pkbf(sg * us, 0.f);
    float o[4] = {0.f, 0.f, 0.f, 0.f};
#pragma unroll
    for (int p = 0; p < 8; ++p) {
      u32 hp = __builtin_amdgcn_readlane(hb, 2 * p) | (__builtin_amdgcn_readlane(hb, 2 * p + 1) << 16);
#pragma unroll
      for (int j = 0; j < 2; ++j) {
        o[2 * j] = dot2(__builtin_amdgcn_perm(dn[2 * p + 1][j], dn[2 * p][j], 0x05040100u), hp, o[2 * j]);
        o[2 * j + 1] = dot2(__builtin_amdgcn_perm(dn[2 * p + 1][j], dn[2 * p][j], 0x07060302u), hp, o[2 * j + 1]);
      }
    }
    ((f32x4*)(ws + WS_PART))[cs * 256 + mt] = f32x4{o[0], o[1], o[2], o[3]};
  }
}

__global__ __launch_bounds__(512) void k3(const u16* __restrict__ x, const u16* __restrict__ dsk,
                                          const char* __restrict__ ws, u16* __restrict__ out) {
  __shared__ float red[8 * 32];
  const int tid = threadIdx.x, lane = tid & 63;
  const u32 key = *(const u32*)(ws + WS_SEL);
  const int idx = 2047 - (int)(key & 0xffffu);
  if (blockIdx.x < 256) {
    const int blk = blockIdx.x;
    const u32* xp = (const u32*)(x + blk * 8 * D) + tid;
    u32 v[8];
#pragma unroll
    for (int r = 0; r < 8; ++r) v[r] = xp[r * 512];
    const u32x4* yt = (const u32x4*)(ws + WS_XT) + blk * 1024;
    const u32x4 y0 = yt[tid], y1 = yt[512 + tid];
    const u32 dd = ((const u32*)dsk)[tid];
    const f32x2* st = (const f32x2*)(ws + WS_STAT) + blk * 8;
    const float d0 = lo16(dd), d1 = hi16(dd);
    u32* op = (u32*)(out + blk * 8 * D) + tid;
#pragma unroll
    for (int r = 0; r < 8; ++r) {
      const f32x2 s = st[r];
      const float xa = lo16(v[r]), xb = hi16(v[r]);
      u32 t = pkbf(xa - s[0], xb - s[0]);
      t = pkbf(lo16(t) * s[1], hi16(t) * s[1]);
      const float na = lo16(t), nb = hi16(t);
      t = pkbf(na * d0, nb * d1);
      const float ya = (r & 1) ? hi16(y0[r >> 1]) : lo16(y0[r >> 1]);
      const float yb = (r & 1) ? hi16(y1[r >> 1]) : lo16(y1[r >> 1]);
      t = pkbf(ya + lo16(t), yb + hi16(t));
      t = pkbf(xa + lo16(t), xb + hi16(t));
      t = pkbf(lo16(t) + na, hi16(t) + nb);
      if (blk * 8 + r != idx) op[r * 512] = t;
    }
  } else {
    const int cb = blockIdx.x - 256;
    const int cq = tid & 7, pg = tid >> 3;
    const f32x4* pp = (const f32x4*)(ws + WS_PART) + cb * 8 + cq;
    f32x4 a = pp[pg * 256] + pp[(pg + 64) * 256];
    f32x4 b = pp[(pg + 128) * 256];
    if (pg < 32) b += pp[(pg + 192) * 256];
    a += b;
    float t = sw16(sw32(a[0], a[2]), sw32(a[1], a[3]));
    t += dppf<DPP_ROR8>(t);
    if ((lane & 15) < 8) red[(tid >> 6) * 32 + (lane & 7) * 4 + (lane >> 4)] = t;
    __syncthreads();
    if (tid < 32) {
      float y = 0.f;
#pragma unroll
      for (int w = 0; w < 8; ++w) y += red[w * 32 + tid];
      const int d = cb * 32 + tid;
      u32 kb16 = key >> 16;
      kb16 = (kb16 & 0x8000u) ? (kb16 & 0x7fffu) : (~kb16 & 0xffffu);
      const float r = __uint_as_float(kb16 << 16);
      const float rs = rbf(1.f / (1.f + expf(-r)));
      const float upd = rbf(rbf(y) * rs);
      const float xn = bf2f(((const u16*)(ws + WS_CAND))[(idx >> 3) * 1024 + d]);
      const int pos = (d & 1) * 512 + (d >> 1);
      const float yc = bf2f(((const u16*)(ws + WS_XT))[((idx >> 3) * 1024 + pos) * 8 + (idx & 7)]);
      const float xv = bf2f(x[idx * D + d]);
      float o = rbf(yc + rbf(xn * bf2f(dsk[d])));
      o = rbf(xv + o);
      o = o + rbf(xn + upd);
      out[idx * D + d] = (u16)(pkbf(o, o) & 0xffffu);
    }
  }
}

extern "C" void kernel_launch(void* const* d_in, const int* in_sizes, int n_in,
                              void* d_out, int out_size, void* d_ws, size_t ws_size,
                              hipStream_t stream) {
  const u16* x = (const u16*)d_in[0];
  char* ws = (char*)d_ws;
  k1<<<512, 512, 0, stream>>>(x, (const u16*)d_in[8], (const u16*)d_in[1], (const u16*)d_in[2], (const u16*)d_in[3],
                              (const u16*)d_in[4], (const u16*)d_in[5], ws);
  k2<<<256, 768, 0, stream>>>((const u16*)d_in[9], (const u16*)d_in[10], (const u16*)d_in[11], ws);
  k3<<<256 + 32, 512, 0, stream>>>(x, (const u16*)d_in[6], ws, (u16*)d_out);
}
```

```cpp
#include <hip/hip_runtime.h>
#include <stdint.h>

typedef unsigned short u16;
typedef unsigned int u32;
typedef __attribute__((ext_vector_type(2))) u32 u32x2;
typedef __attribute__((ext_vector_type(4))) u32 u32x4;
typedef __attribute__((ext_vector_type(2))) float f32x2;
typedef __attribute__((ext_vector_type(4))) float f32x4;
typedef __attribute__((ext_vector_type(16))) float f32x16;
typedef __attribute__((ext_vector_type(8))) __bf16 bf16x8;
typedef __attribute__((ext_vector_type(2))) __bf16 bf16x2;
typedef __attribute__((ext_vector_type(8))) _Float16 f16x8;
typedef __attribute__((ext_vector_type(2))) _Float16 f16x2;
typedef u32x2 __attribute__((aligned(2))) u32x2_u;

constexpr int L = 2048, D = 1024, NM = 128, F = 3584;

#define STP 16
#define PF_EARLY 1
#define DEVI __device__ __forceinline__
#define STAMP(k, i)
#define WAITV()
DEVI float lo16(u32 u) { return __uint_as_float(u << 16); }
DEVI float hi16(u32 u) { return __uint_as_float(u & 0xffff0000u); }
DEVI float bf2f(u16 v) { return __uint_as_float(((u32)v) << 16); }
DEVI u32 pkbf(float a, float b) { f32x2 v = {a, b}; return __builtin_bit_cast(u32, __builtin_convertvector(v, bf16x2)); }
DEVI u32 pkh(float a, float b) { f32x2 v = {a, b}; return __builtin_bit_cast(u32, __builtin_convertvector(v, f16x2)); }
DEVI float rbf(float a) { return lo16(pkbf(a, a)); }

template <int CTRL> DEVI float dppf(float v) {
  return __uint_as_float(__builtin_amdgcn_update_dpp(0u, __float_as_uint(v), CTRL, 0xf, 0xf, true));
}
template <int CTRL> DEVI u32 dppu(u32 v) { return __builtin_amdgcn_update_dpp(0u, v, CTRL, 0xf, 0xf, true); }
constexpr int DPP_X1 = 0xB1, DPP_X2 = 0x4E, DPP_HMIR = 0x141, DPP_MIR = 0x140, DPP_ROR4 = 0x124, DPP_ROR8 = 0x128, DPP_WSHR1 = 0x138;
DEVI float row_allsum(float v) {
  v += dppf<DPP_X1>(v); v += dppf<DPP_X2>(v); v += dppf<DPP_HMIR>(v); v += dppf<DPP_MIR>(v); return v;
}
DEVI float sw32(float a, float b) {
  auto r = __builtin_amdgcn_permlane32_swap(__float_as_uint(a), __float_as_uint(b), false, false);
  return __uint_as_float(r[0]) + __uint_as_float(r[1]);
}
DEVI float sw16(float a, float b) {
  auto r = __builtin_amdgcn_permlane16_swap(__float_as_uint(a), __float_as_uint(b), false, false);
  return __uint_as_float(r[0]) + __uint_as_float(r[1]);
}
DEVI u32 umax32(u32 a, u32 b) { return a > b ? a : b; }
DEVI u32 wave_umax(u32 v) {
  v = umax32(v, dppu<DPP_X1>(v)); v = umax32(v, dppu<DPP_X2>(v)); v = umax32(v, dppu<DPP_HMIR>(v)); v = umax32(v, dppu<DPP_MIR>(v));
  u32 a = __builtin_amdgcn_readlane(v, 0), b = __builtin_amdgcn_readlane(v, 16), c = __builtin_amdgcn_readlane(v, 32), d = __builtin_amdgcn_readlane(v, 48);
  return umax32(umax32(a, b), umax32(c, d));
}
DEVI float rdl(float v, int l) { return __uint_as_float(__builtin_amdgcn_readlane(__float_as_uint(v), l)); }

constexpr int I3E = 127, I3O = 223, I2A = 255, I2B = 319, I2C = 383, I2D = 447, LM = 975, IX = 1016;
constexpr int ZIDX = 24 * 32;
constexpr int rnd_bf16_int(int l) {
  if (l < 256) return l;
  int s = 0; while ((l >> s) >= 256) ++s;
  int q = l >> s, rem = l & ((1 << s) - 1), half = 1 << (s - 1);
  if (rem > half || (rem == half && (q & 1))) ++q;
  return q << s;
}
constexpr int pos_idx(int p) {
  if (p == 0) return 0;
  int row = 0, alpha = 0;
  if (p <= 256) { alpha = ((p - 1) & 7) + 1; row = alpha; }
  else if (p <= 512) { int o = ((p - 258) & 7) >> 1; alpha = 258 + 2 * o; row = 9 + o; }
  else if (p <= 1024) { int i = (p - 516) >> 8; int o = ((p - 516) & 7) >> 2; alpha = 516 + 256 * i + 4 * o; row = 13 + 2 * i + o; }
  else { int i = (p - 1032) >> 8; alpha = 1032 + 256 * i; row = 17 + i; }
  return row * 32 + (p - alpha) / 8;
}
struct Lut { u16 v[1024]; };
constexpr Lut make_lut() {
  Lut t{};
  for (int i = 0; i < 1024; ++i) t.v[i] = ZIDX;
  for (int m = 64; m <= 127; ++m) { t.v[I3E - m] = pos_idx(16 * (m + 1)); t.v[I3O - m] = pos_idx(16 * m + 8); }
  for (int m = 32; m <= 63; ++m) {
    t.v[I2A - m] = pos_idx(16 * (m + 1));
    if (m <= 62) t.v[I2B - m] = pos_idx(16 * (m + 1) + 4);
    t.v[I2C - m] = pos_idx(16 * m + 8);
    t.v[I2D - m] = pos_idx(16 * m + 12);
  }
  for (int l = 0; l <= 517; ++l) t.v[LM - l] = pos_idx(rnd_bf16_int(l));
  t.v[IX] = pos_idx(1024);
  for (int i = 0; i < 1024; ++i) t.v[i] *= 4;
  return t;
}
__device__ const Lut g_lut = make_lut();

constexpr size_t WS_XT = 0;
constexpr size_t WS_KT = 4u << 20;
constexpr size_t WS_PART = (6u << 20) + 4096;
constexpr size_t WS_CAND = WS_PART + 224 * 4096;
constexpr size_t WS_STAT = WS_CAND + 256 * 2048;
constexpr size_t WS_KEYS = WS_STAT + 2048 * 8;
constexpr size_t WS_SEL = WS_KEYS + 1024;
constexpr size_t WS_GRAN = WS_SEL + 1024;

DEVI float dot2(u32 a, u32 b, float c) {
  return __builtin_amdgcn_fdot2_f32_bf16(__builtin_bit_cast(bf16x2, a), __builtin_bit_cast(bf16x2, b), c, false);
}
DEVI f32x2 rot(f32x2 b) { return f32x2{-b[1], b[0]}; }
DEVI f32x2 cmul(f32x2 a, f32x2 b, f32x2 bs) {
  return __builtin_elementwise_fma(f32x2{a[1], a[1]}, bs, f32x2{a[0], a[0]} * b);
}
DEVI f32x2 cexp2(float lg, float rev) {
  float m = __builtin_amdgcn_exp2f(lg);
  return f32x2{m * __builtin_amdgcn_cosf(rev), m * __builtin_amdgcn_sinf(rev)};
}
DEVI float exp_acc(float x) {
  const float t = x * 1.44269502162933349609375f;
  const float e = __builtin_fmaf(x, 1.44269502162933349609375f, -t) + x * 1.925963033500011e-8f;
  const float r = __builtin_amdgcn_exp2f(t);
  return __builtin_fmaf(r, e * 0.6931471805599453f, r);
}
template <typename R> DEVI void st16(R rsrc, u32 off, u32x4 v) { __builtin_amdgcn_raw_buffer_store_b128(v, rsrc, off, 0, STP); }
template <typename R> DEVI void st8(R rsrc, u32 off, u32x2 v) { __builtin_amdgcn_raw_buffer_store_b64(v, rsrc, off, 0, STP); }
#define NTL(T, p) __builtin_nontemporal_load((const T*)(p))
typedef __attribute__((ext_vector_type(4))) short s16x4;
DEVI u32x2 ds_tr(const char* p) {
  typedef __attribute__((address_space(3))) s16x4* lp;
  return __builtin_bit_cast(u32x2, __builtin_amdgcn_ds_read_tr16_b64_v4i16((lp)p));
}

constexpr int KG_ROW = 272, KG_WAVE = 32 * KG_ROW;
constexpr int LN_ROW = 2048 + 64;

__global__ __launch_bounds__(256) void k1(const u16* __restrict__ x, const u16* __restrict__ wr,
                                          const u16* __restrict__ lar, const u16* __restrict__ aim,
                                          const u16* __restrict__ cre, const u16* __restrict__ cim,
                                          const u16* __restrict__ ldt, const u16* __restrict__ wg,
                                          const u16* __restrict__ wu, char* __restrict__ ws) {
  __shared__ __attribute__((aligned(16))) char smem[4 * KG_WAVE];
  const int tid = threadIdx.x, lane = tid & 63, wv = __builtin_amdgcn_readfirstlane(tid >> 6);
  const auto wsr = __builtin_amdgcn_make_buffer_rsrc(ws, 0, 0x7fffffff, 0x00020000);
  STAMP(0, 0);
  if (blockIdx.x < 256) {
    const int blk = blockIdx.x;
    const int lr = tid >> 5, lc = tid & 31;
    const u32x4* xp = (const u32x4*)(x + (blk * 8 + lr) * D) + lc;
    u32x4 v[4], w[4];
#pragma unroll
    for (int i = 0; i < 4; ++i) v[i] = NTL(u32x4, xp + i * 32);
#pragma unroll
    for (int i = 0; i < 4; ++i) w[i] = ((const u32x4*)wr)[i * 32 + lc];
    WAITV(); STAMP(0, 1);
    float s1 = 0.f, s2 = 0.f;
#pragma unroll
    for (int i = 0; i < 4; ++i)
#pragma unroll
      for (int j = 0; j < 4; ++j) { s1 = dot2(v[i][j], 0x3f803f80u, s1); s2 = dot2(v[i][j], v[i][j], s2); }
    float t = row_allsum(sw16(s1, s2));
    auto sw = __builtin_amdgcn_permlane16_swap(__float_as_uint(t), __float_as_uint(t), false, false);
    const float mean = __uint_as_float(sw[0]) * (1.f / D);
    float var = __uint_as_float(sw[1]) * (1.f / D) - mean * mean;
    if (__builtin_amdgcn_ballot_w64(!(mean * mean <= 16.f * var)) != 0) {
      float q = 0.f;
#pragma unroll
      for (int i = 0; i < 4; ++i)
#pragma unroll
        for (int j = 0; j < 4; ++j) { float a = lo16(v[i][j]) - mean, b = hi16(v[i][j]) - mean; q += a * a + b * b; }
      q = row_allsum(q);
      var = sw16(q, q) * (1.f / D);
    }
    const float m = rbf(mean);
    const float sd = rbf(__builtin_amdgcn_sqrtf(rbf(rbf(var) + 1e-5f)));
    const float rs = __builtin_amdgcn_rcpf(sd);
    float dt = 0.f;
    char* lw = smem + lr * LN_ROW + lc * 16;
#pragma unroll
    for (int i = 0; i < 4; ++i) {
      u32x4 z;
#pragma unroll
      for (int j = 0; j < 4; ++j) {
        const f32x2 df = f32x2{lo16(v[i][j]), hi16(v[i][j])} - m;
        const u32 dd = pkbf(df[0], df[1]);
        const f32x2 zf = f32x2{lo16(dd), hi16(dd)} * rs;
        z[j] = pkbf(zf[0], zf[1]);
        dt = dot2(z[j], w[i][j], dt);
      }
      *(u32x4*)(lw + i * 512) = z;
    }
    dt = row_allsum(dt);
    dt = sw16(dt, dt);
    u32 b = pkbf(dt, dt) & 0xffffu;
    b = (b & 0x8000u) ? (~b & 0xffffu) : (b | 0x8000u);
    u32* rk = (u32*)(smem + 8 * LN_ROW);
    if (lc == 0) {
      rk[lr] = (b << 16) | (u32)(2047 - (blk * 8 + lr));
      ((f32x2*)(ws + WS_STAT))[blk * 8 + lr] = f32x2{m, rs};
    }
    STAMP(0, 2);
    __syncthreads();
    {
      const char* tp = smem + ((lane >> 2) & 3) * LN_ROW + (wv * 256 + (lane >> 4) * 16 + (lane & 3) * 4) * 2;
      const u32 xo = WS_XT + (blk * 1024 + (lane & 3) * 256 + wv * 64 + (lane >> 2)) * 16;
#pragma unroll
      for (int it = 0; it < 4; ++it) {
        const u32x2 a = ds_tr(tp + it * 128), c = ds_tr(tp + it * 128 + 4 * LN_ROW);
        st16(wsr, xo + it * 256, u32x4{a[0], a[1], c[0], c[1]});
      }
    }
    const u32x4 ka = *(const u32x4*)rk, kb = *(const u32x4*)(rk + 4);
    const u32 key = umax32(umax32(umax32(ka[0], ka[1]), umax32(ka[2], ka[3])), umax32(umax32(kb[0], kb[1]), umax32(kb[2], kb[3])));
    const int rbest = (2047 - (int)(key & 0xffffu)) & 7;
    ((u32x2*)(ws + WS_CAND))[blk * 256 + tid] = *(const u32x2*)(smem + rbest * LN_ROW + tid * 8);
    if (tid == 0) ((u32*)(ws + WS_KEYS))[blk] = key;
    if (tid < 64 && blk < 224) ((u32x4*)(ws + WS_GRAN))[blk * 64 + tid] = u32x4{0u, 0u, 0u, 0u};
    STAMP(0, 3); WAITV(); STAMP(0, 4);
  } else {
    const int kb = blockIdx.x - 256;
    const int pos = kb * 2 + (wv >> 1);
    const int d = 4 * (pos & 255) + (pos >> 8);
    const int n = d * NM + (wv & 1) * 64 + lane;
    const u16 h_lar = lar[n], h_aim = aim[n], h_cr = cre[n], h_ci = cim[n], h_ldt = ldt[d];
    const u32x4 lut = *(const u32x4*)(g_lut.v + (tid & 127) * 8);
    u32x4 pf[7];
    const u16* pfp = (((kb >> 3) & 1) ? wu : wg) + (size_t)(32 * (kb >> 4) + (tid >> 3)) * F + 64 * (kb & 7) + 8 * (tid & 7);
#pragma unroll
    for (int i = 0; i < 7; ++i) pf[i] = NTL(u32x4, pfp + i * 512);
    const float f_lar = bf2f(h_lar), f_aim = bf2f(h_aim), f_cr = bf2f(h_cr), f_ci = bf2f(h_ci), f_ldt = bf2f(h_ldt);
    asm volatile("" :: "v"(f_lar), "v"(f_aim), "v"(f_cr), "v"(f_ci), "v"(f_ldt), "v"(lut));
    STAMP(0, 1);
    const float dtv = rbf(exp_acc(f_ldt));
    const float zr = -rbf(exp_acc(f_lar)) * dtv;
    const float zi = f_aim * dtv;
    const float lg = zr * 1.44269504088896341f;
    const float c_hi = 0.15915494309189535f, c_lo = 6.4206382e-9f;
    const float p = zi * c_hi;
    const float tl = __builtin_fmaf(zi, c_hi, -p) + zi * c_lo;
    const float th = p - __builtin_rintf(p);
    const f32x2 e1 = cexp2(lg, th + tl);
    const f32x2 e8 = cexp2(lg * 8.f, __builtin_amdgcn_fractf(th * 8.f) + tl * 8.f);
    const f32x2 e256 = cexp2(lg * 256.f, __builtin_amdgcn_fractf(th * 256.f) + tl * 256.f);
    const float cr2 = 2.f * f_cr * dtv, ci2 = 2.f * f_ci * dtv;
    u32 mx = umax32(__float_as_uint(cr2) & 0x7fffffffu, __float_as_uint(ci2) & 0x7fffffffu);
    mx = wave_umax(mx);
    int ex = (int)(mx >> 23); ex = ex < 1 ? 1 : (ex > 253 ? 253 : ex);
    const float sinv = __uint_as_float((u32)(254 - ex) << 23);
    char* base = smem + wv * KG_WAVE;
    u32* wp = (u32*)(base + lane * 4);
#define WROW(rw, cc) wp[(rw) * (KG_ROW / 4)] = pkh((cc)[0], (cc)[1])
    wp[0] = 0x00003c00u;
    {
      const f32x2 r1 = rot(e1), r256 = rot(e256);
      const f32x2 e2 = cmul(e1, e1, r1), e3 = cmul(e2, e1, r1), e4 = cmul(e3, e1, r1), e5 = cmul(e4, e1, r1), e6 = cmul(e5, e1, r1), e7 = cmul(e6, e1, r1);
      WROW(1, e1); WROW(2, e2); WROW(3, e3); WROW(4, e4); WROW(5, e5); WROW(6, e6); WROW(7, e7); WROW(8, e8);
      f32x2 t = cmul(e8, e256, r256); WROW(12, t);
      f32x2 s = cmul(e4, e256, r256); WROW(10, s);
      t = cmul(t, e256, r256); WROW(14, t);
      s = cmul(s, e256, r256); WROW(13, s);
      t = cmul(t, e256, r256); WROW(16, t);
      s = cmul(s, e256, r256); WROW(15, s);
      t = cmul(t, e256, r256); WROW(17, t);
      s = cmul(e2, e256, r256); WROW(9, s);
      t = cmul(t, e256, r256); WROW(18, t);
      s = cmul(e6, e256, r256); WROW(11, s);
      t = cmul(t, e256, r256); WROW(19, t); t = cmul(t, e256, r256); WROW(20, t);
    }
    const char* rp = base + (lane & 31) * KG_ROW + (lane >> 5) * 16;
    f16x8 af[8];
    asm volatile("" ::: "memory");
#pragma unroll
    for (int s = 0; s < 8; ++s) af[s] = *(const f16x8*)(rp + s * 32);
    asm volatile("" ::: "memory");
    STAMP(0, 2);
    {
      const f32x2 c8 = {e8[0], -e8[1]}, r8 = rot(c8);
      const f32x2 c16 = cmul(c8, c8, r8), r16 = rot(c16);
      f32x2 g0 = {cr2 * sinv, -ci2 * sinv};
      f32x2 g1 = cmul(g0, c8, r8);
#pragma unroll
      for (int k = 0; k < 16; ++k) {
        WROW(2 * k, g0); WROW(2 * k + 1, g1);
        if (k < 15) { g0 = cmul(g0, c16, r16); g1 = cmul(g1, c16, r16); }
      }
    }
    asm volatile("" ::: "memory");
    f32x16 acc = {};
#pragma unroll
    for (int s = 0; s < 8; ++s) {
      f16x8 bfr = *(const f16x8*)(rp + s * 32);
      acc = __builtin_amdgcn_mfma_f32_32x32x16_f16(af[s], bfr, acc, 0, 0, 0);
    }
    asm volatile("" ::: "memory");
    STAMP(0, 3);
    float* kp = (float*)base + (lane >> 5) * 128 + (lane & 31);
#pragma unroll
    for (int r = 0; r < 12; ++r) kp[((r & 3) + 8 * (r >> 2)) * 32] = __builtin_amdgcn_ldexpf(acc[r], ex - 127);
    if (lane == 0) kp[ZIDX] = 0.f;
    __syncthreads();
    const char* k0 = smem + (wv & ~1) * KG_WAVE;
    u32 o[4];
#pragma unroll
    for (int j = 0; j < 4; ++j) {
      const float* pa = (const float*)(k0 + (lut[j] & 0xffffu));
      const float* pb = (const float*)(k0 + (lut[j] >> 16));
      o[j] = pkbf(pa[0] + pa[KG_WAVE / 4], pb[0] + pb[KG_WAVE / 4]);
    }
    st16(wsr, WS_KT + (kb * 256 + tid) * 16, u32x4{o[0], o[1], o[2], o[3]});
    STAMP(0, 4);
#pragma unroll
    for (int i = 0; i < 7; ++i) asm volatile("" :: "v"(pf[i]));
    WAITV(); STAMP(0, 5);
  }
}

constexpr int U_STRIDE = 4640;
constexpr int KT0 = 4 * U_STRIDE;
constexpr int YW0 = KT0 + 4 * 8192;
constexpr int YW_STRIDE = 6592;
constexpr int MISC0 = YW0 + 4 * YW_STRIDE;
constexpr int K2_LDS = MISC0 + 64 + 2048;

DEVI u32x4 make_box(int ce, int w, int rho, int kg, u32 val) {
  int base = 8 * kg - 16 - rho - ce;
  int sh = 22 + base; sh = sh < 0 ? 0 : (sh > 31 ? 31 : sh);
  u32 bits = (((1u << w) - 1u) << 22) >> sh;
  u32x4 o;
#pragma unroll
  for (int p = 0; p < 4; ++p) o[p] = __umul24(__umul24((bits >> (2 * p)) & 3u, 0x8001u) & 0x10001u, val);
  return o;
}
DEVI bf16x8 asbf(u32x4 v) { return __builtin_bit_cast(bf16x8, v); }
DEVI bf16x8 ld2x64(const char* p, int off2) {
  u32x2 a = *(const u32x2*)p, b = *(const u32x2*)(p + off2);
  return asbf(u32x4{a[0], a[1], b[0], b[1]});
}
DEVI f32x4 mf16(bf16x8 a, bf16x8 b, f32x4 c) { return __builtin_amdgcn_mfma_f32_16x16x32_bf16(a, b, c, 0, 0, 0); }
DEVI bf16x8 packx(f32x4 a, f32x4 b) { return asbf(u32x4{pkbf(a[0], a[1]), pkbf(a[2], a[3]), pkbf(b[0], b[1]), pkbf(b[2], b[3])}); }
DEVI void lds_wait_ge(volatile u32* p, u32 v) {
  for (int i = 0; i < (1 << 22) && __builtin_amdgcn_readfirstlane(*p) < v; ++i) __builtin_amdgcn_s_sleep(1);
  asm volatile("" ::: "memory");
}
DEVI void lds_signal(u32* p) {
  asm volatile("s_waitcnt lgkmcnt(0)" ::: "memory");
  if ((threadIdx.x & 63) == 0) __hip_atomic_fetch_add(p, 1u, __ATOMIC_RELAXED, __HIP_MEMORY_SCOPE_WORKGROUP);
}

__global__ __launch_bounds__(768, 3) void k2(const u16* __restrict__ wg, const u16* __restrict__ wu,
                                          const u16* __restrict__ wd, char* __restrict__ ws) {
  __shared__ __attribute__((aligned(16))) char smem[K2_LDS];
  const int tid = threadIdx.x, lane = tid & 63, wv = __builtin_amdgcn_readfirstlane(tid >> 6);
  const int bx = blockIdx.x & 7, bk = blockIdx.x >> 3;
  u32* flg = (u32*)(smem + MISC0);
  volatile u32* flags = flg;
  const auto wsr = __builtin_amdgcn_make_buffer_rsrc(ws, 0, 0x7fffffff, 0x00020000);
  STAMP(1, 0);
  if (wv < 8) {
    const int B = bx * 32 + bk;
    const int c = wv & 3;
    u32x4* xt = (u32x4*)(ws + WS_XT) + B * 4;
    const int stb = tid >> 2, sc = tid & 3;
    u32x4 u0 = xt[stb * 1024 + sc], u1 = xt[(stb + 128) * 1024 + sc];
    const u16* kt = (const u16*)(ws + WS_KT) + B * 4096;
    u32x2 kv[8];
#pragma unroll
    for (int i = 0; i < 8; ++i) kv[i] = *(const u32x2_u*)(kt + i * 512 + tid);
    if (tid < 48) *(u32*)(smem + (tid / 12) * U_STRIDE + (tid % 12) * 4) = 0u;
    else if (tid < 64) flags[tid - 48] = 0u;
    else if (tid < 96) *(float*)(smem + YW0 + ((tid - 64) >> 3) * YW_STRIDE + (tid & 7) * 4) = 0.f;
    asm volatile("s_waitcnt lgkmcnt(0)" ::: "memory");
    __builtin_amdgcn_s_barrier();
    asm volatile("" ::: "memory");
    char* sdst = smem + sc * U_STRIDE + (stb + (stb >> 3) + 3) * 16;
    const char* ub = smem + c * U_STRIDE;
    const char* kb = smem + KT0 + c * 8192;
    if (wv < 4) {
      *(u32x4*)sdst = u0; *(u32x4*)(sdst + 144 * 16) = u1;
#pragma unroll
      for (int i = 0; i < 8; ++i) *(u32x2*)(smem + KT0 + (i * 512 + tid) * 8) = kv[i];
      lds_signal(flg + 6);
      STAMP(1, 7);
      lds_wait_ge(flags + 6, 8);
      STAMP(1, 1);
      const int a = lane & 31, h = lane >> 5;
      u32x4 Bq[4];
#pragma unroll
      for (int q = 0; q < 4; ++q) Bq[q] = *(const u32x4*)(ub + (9 * a + 2 * q + h + 3) * 16);
      u32 msk = lane == 32 ? 0u : ~0u;
      asm volatile("" : "+v"(msk));
      const char* ap = kb + (LM - 16 * 33 - a + 8 * h) * 8;
      f32x16 acc0 = {}, acc1 = {};
      bf16x8 A[37];
#pragma unroll
      for (int s = 0; s < 37; ++s) {
        const int q = 3 - (s & 3);
        if (s <= 34) {
          A[s] = ld2x64(ap + (33 - (s - 1)) * 128, 32);
          acc1 = __builtin_amdgcn_mfma_f32_32x32x16_bf16(A[s], asbf(Bq[q]), acc1, 0, 0, 0);
        }
        if (s >= 2) acc0 = __builtin_amdgcn_mfma_f32_32x32x16_bf16(A[s - 2], asbf(Bq[q]), acc0, 0, 0, 0);
        if (s + 4 < 37) {
#pragma unroll
          for (int j = 0; j < 4; ++j) Bq[q][j] = dppu<DPP_WSHR1>(Bq[q][j]) & msk;
        }
      }
      STAMP(1, 2);
      lds_wait_ge(flags + c, 1);
      STAMP(1, 3);
      if (a >= 8) {
        const char* yp = smem + YW0 + c * YW_STRIDE + ((a - 8) * 68 + 4 * h) * 4;
#pragma unroll
        for (int g = 0; g < 4; ++g) {
          f32x4 y0 = *(const f32x4*)(yp + g * 32), y1 = *(const f32x4*)(yp + 128 + g * 32);
#pragma unroll
          for (int i = 0; i < 4; ++i) { acc0[4 * g + i] += y0[i]; acc1[4 * g + i] += y1[i]; }
        }
      }
      char* op = smem + c * U_STRIDE + (9 * a + 3) * 16 + 8 * h;
#pragma unroll
      for (int g = 0; g < 4; ++g) {
        *(u32x2*)(op + g * 16) = u32x2{pkbf(acc0[4 * g], acc0[4 * g + 1]), pkbf(acc0[4 * g + 2], acc0[4 * g + 3])};
        *(u32x2*)(op + 64 + g * 16) = u32x2{pkbf(acc1[4 * g], acc1[4 * g + 1]), pkbf(acc1[4 * g + 2], acc1[4 * g + 3])};
      }
      lds_signal(flg + 4);
    } else {
      __builtin_amdgcn_s_setprio(2);
      const int rho = lane & 15, kg = lane >> 4;
      const u32x4 bxs[6] = {make_box(-13, 5, rho, kg, 0x3f80u), make_box(-16, 3, rho, kg, 0x3f80u), make_box(-5, 5, rho, kg, 0x3f80u),
                            make_box(-8, 3, rho, kg, 0x3f80u), make_box(-15, 9, rho, kg, 0x3f80u), make_box(-6, 7, rho, kg, 0x3f80u)};
      u32x4 bxx = make_box(-15, 2, rho, kg, 1u);
      *(u32x4*)sdst = u0; *(u32x4*)(sdst + 144 * 16) = u1;
#pragma unroll
      for (int i = 0; i < 8; ++i) *(u32x2*)(smem + KT0 + (i * 512 + tid) * 8) = kv[i];
      lds_signal(flg + 6);
      STAMP(1, 7);
      lds_wait_ge(flags + 6, 8);
      STAMP(1, 1);
      bf16x8 uf[6];
      {
        const int pc = 2 * rho - 2 + kg;
        const char* up = ub + (pc + (pc >> 3) + 3) * 16;
#pragma unroll
        for (int b = 0; b < 6; ++b) uf[b] = asbf(*(const u32x4*)(up + b * 576));
      }
      const char* kp = kb + (4 * kg - rho) * 8;
      const f32x4 z = {};
      f32x4 Y[6];
      f32x4 X[2][6];
      constexpr int IC[6] = {I2A - 32, I2B - 32, I2C - 32, I2D - 32, I3E - 64, I3O - 64};
#pragma unroll
      for (int b = 0; b < 6; ++b) X[0][b] = mf16(uf[b], asbf(bxs[0]), z);
      {
        const u32 kx = *(const u16*)(kb + IX * 8);
        const int pc = 2 * rho + kg;
        const char* up = ub + (pc + (pc >> 3) + 3) * 16;
        int pm = pc - 32; pm = pm < -2 ? -2 : pm;
        const char* um = ub + (pm + (pm >> 3) + 3) * 16;
#pragma unroll
        for (int p = 0; p < 4; ++p) bxx[p] = __umul24(bxx[p], kx);
        Y[0] = z;
        Y[1] = mf16(asbf(*(const u32x4*)um), asbf(bxx), z);
#pragma unroll
        for (int b = 0; b < 4; ++b) Y[2 + b] = mf16(asbf(*(const u32x4*)(up + b * 576)), asbf(bxx), z);
      }
#pragma unroll
      for (int cl = 0; cl < 6; ++cl) {
        const int ns = cl < 4 ? 3 : 2, t0 = cl < 4 ? 0 : 2;
        bf16x8 Af[4], Bf[3];
#pragma unroll
        for (int i = 0; i < 4; ++i) Af[i] = ld2x64(kp + (IC[cl] - 16 * i) * 8, 128);
        if (cl < 5) {
#pragma unroll
          for (int b = 0; b < (cl + 1 < 4 ? 6 : 4); ++b) X[(cl + 1) & 1][b] = mf16(uf[b], asbf(bxs[cl + 1]), z);
        }
#pragma unroll
        for (int S = 0; S < ns; ++S) Bf[S] = packx(X[cl & 1][2 * S], X[cl & 1][2 * S + 1]);
#pragma unroll
        for (int S = 0; S < ns; ++S)
#pragma unroll
          for (int i = 0; i < 4; ++i) if (t0 + 2 * S + i < 6) Y[t0 + 2 * S + i] = mf16(Af[i], Bf[S], Y[t0 + 2 * S + i]);
      }
      STAMP(1, 2);
      float* yw = (float*)(smem + YW0 + c * YW_STRIDE);
#pragma unroll
      for (int i = 0; i < 4; ++i) {
        const int u = 64 * kg + 16 * i + rho + 5;
        float* q = yw + u + 4 * (u >> 6);
#pragma unroll
        for (int T = 0; T < 6; ++T) q[T * 272] = Y[T][i];
      }
      lds_signal(flg + c);
      __builtin_amdgcn_s_setprio(0);
    }
    STAMP(1, 4);
    lds_wait_ge(flags + 4, 4);
    STAMP(1, 5);
    st16(wsr, WS_XT + (B * 4 + stb * 1024 + sc) * 16, *(const u32x4*)sdst);
    st16(wsr, WS_XT + (B * 4 + (stb + 128) * 1024 + sc) * 16, *(const u32x4*)(sdst + 144 * 16));
    WAITV(); STAMP(1, 6);
  } else {
    const int mt = tid - 512;
    __builtin_amdgcn_s_barrier();
    if (bk >= 28) return;
    __builtin_amdgcn_s_setprio(3);
    const int lg = 8 * (bk >> 2) + bx, kq = bk & 3, cs = 4 * lg + kq;
    const u32x4 kk = ((const u32x4*)(ws + WS_KEYS))[lane];
    const int rg = mt >> 3, q = mt & 7;
    u32x4 g[8], u[8];
    u32x2 dn[16];
    {
      const u16* pg = wg + (size_t)(kq * 256 + 8 * rg) * F + 64 * lg + 8 * q;
      const u16* pu = wu + (size_t)(kq * 256 + 8 * rg) * F + 64 * lg + 8 * q;
#pragma unroll
      for (int i = 0; i < 8; ++i) { g[i] = NTL(u32x4, pg + i * F); u[i] = NTL(u32x4, pu + i * F); }
    }
    __builtin_amdgcn_sched_barrier(0);
    const u32 key = wave_umax(umax32(umax32(kk[0], kk[1]), umax32(kk[2], kk[3])));
    const int idx = 2047 - (int)(key & 0xffffu);
    const u32x4 xv = *(const u32x4*)(ws + WS_CAND + (idx >> 3) * 2048 + kq * 512 + rg * 16);
    STAMP(1, 1);
    __builtin_amdgcn_sched_barrier(0);
    const u16* pd = wd + (size_t)(16 * cs) * D + 4 * mt;
#pragma unroll
    for (int j = 0; j < 16; ++j) dn[j] = NTL(u32x2, pd + j * D);
    __builtin_amdgcn_sched_barrier(0);
    STAMP(1, 2);
    if (blockIdx.x == 0 && mt == 0) *(u32*)(ws + WS_SEL) = key;
    float s[16];
#pragma unroll
    for (int i = 0; i < 16; ++i) s[i] = 0.f;
#pragma unroll
    for (int p = 0; p < 4; ++p) {
#pragma unroll
      for (int j = 0; j < 4; ++j) {
        s[2 * j] = dot2(__builtin_amdgcn_perm(g[2 * p + 1][j], g[2 * p][j], 0x05040100u), xv[p], s[2 * j]);
        s[2 * j + 1] = dot2(__builtin_amdgcn_perm(g[2 * p + 1][j], g[2 * p][j], 0x07060302u), xv[p], s[2 * j + 1]);
        s[8 + 2 * j] = dot2(__builtin_amdgcn_perm(u[2 * p + 1][j], u[2 * p][j], 0x05040100u), xv[p], s[8 + 2 * j]);
        s[8 + 2 * j + 1] = dot2(__builtin_amdgcn_perm(u[2 * p + 1][j], u[2 * p][j], 0x07060302u), xv[p], s[8 + 2 * j + 1]);
      }
    }
    STAMP(1, 3);
#pragma unroll
    for (int i = 0; i < 8; ++i) s[i] = sw32(s[i], s[i + 8]);
#pragma unroll
    for (int i = 0; i < 4; ++i) { float t = sw16(s[i], s[i + 4]); s[i] = t + dppf<DPP_ROR8>(t); }
    float* red = (float*)(smem + MISC0 + 64);
    if ((lane & 8) == 0) *(f32x4*)(red + (wv - 8) * 128 + (lane >> 5) * 64 + 8 * q + ((lane >> 4) & 1) * 4) = f32x4{s[0], s[1], s[2], s[3]};
    lds_signal(flg + 5);
    typedef unsigned long long u64;
    u64* gran = (u64*)(ws + WS_GRAN);
    if (wv < 10) {
      lds_wait_ge(flags + 5, 4);
      const float v = (red[mt] + red[128 + mt]) + (red[256 + mt] + red[384 + mt]);
      __hip_atomic_store(gran + (lg * 4 + ((mt >> 4) & 3)) * 128 + kq * 32 + (mt >> 6) * 16 + (mt & 15),
                         (1ull << 32) | __float_as_uint(v), __ATOMIC_RELAXED, __HIP_MEMORY_SCOPE_AGENT);
    }
    STAMP(1, 4);
    u64 ga, gb;
    for (int it = 0; it < (1 << 20); ++it) {
      ga = __hip_atomic_load(gran + cs * 128 + lane, __ATOMIC_RELAXED, __HIP_MEMORY_SCOPE_AGENT);
      gb = __hip_atomic_load(gran + cs * 128 + 64 + lane, __ATOMIC_RELAXED, __HIP_MEMORY_SCOPE_AGENT);
      if (__builtin_amdgcn_ballot_w64((u32)(ga >> 32) == 1u && (u32)(gb >> 32) == 1u) == ~0ull) break;
      __builtin_amdgcn_s_sleep(2);
    }
    STAMP(1, 5);
    float tt = __uint_as_float((u32)ga) + __uint_as_float((u32)gb);
    tt = sw32(tt, tt);
    auto sw = __builtin_amdgcn_permlane16_swap(__float_as_uint(tt), __float_as_uint(tt), false, false);
    const float gs = rbf(__uint_as_float(sw[0])), us = rbf(__uint_as_float(sw[1]));
    const float sg = rbf(gs * __builtin_amdgcn_rcpf(1.f + exp_acc(-gs)));
    const u32 hb = pkbf(sg * us, 0.f);
    float o[4] = {0.f, 0.f, 0.f, 0.f};
#pragma unroll
    for (int p = 0; p < 8; ++p) {
      u32 hp = __builtin_amdgcn_readlane(hb, 2 * p) | (__builtin_amdgcn_readlane(hb, 2 * p + 1) << 16);
#pragma unroll
      for (int j = 0; j < 2; ++j) {
        o[2 * j] = dot2(__builtin_amdgcn_perm(dn[2 * p + 1][j], dn[2 * p][j], 0x05040100u), hp, o[2 * j]);
        o[2 * j + 1] = dot2(__builtin_amdgcn_perm(dn[2 * p + 1][j], dn[2 * p][j], 0x07060302u), hp, o[2 * j + 1]);
      }
    }
    st16(wsr, WS_PART + (cs * 256 + mt) * 16, __builtin_bit_cast(u32x4, f32x4{o[0], o[1], o[2], o[3]}));
    STAMP(1, 6); WAITV(); STAMP(1, 7);
  }
}

__global__ __launch_bounds__(256) void k3(const u16* __restrict__ x, const u16* __restrict__ dsk,
                                          const char* __restrict__ ws, u16* __restrict__ out) {
  __shared__ float red[4 * 16];
  const int tid = threadIdx.x, lane = tid & 63;
  STAMP(2, 0);
  if (blockIdx.x < 256) {
    const int blk = blockIdx.x;
    const u32x2* xp = (const u32x2*)(x + blk * 8 * D) + tid;
    u32x2 v[8];
    u32x4 y[4];
    const u32x4* yt = (const u32x4*)(ws + WS_XT) + blk * 1024 + tid;
    const f32x2 st = ((const f32x2*)(ws + WS_STAT))[blk * 8 + (lane & 7)];
#pragma unroll
    for (int r = 0; r < 8; ++r) v[r] = NTL(u32x2, xp + r * 256);
#pragma unroll
    for (int i = 0; i < 4; ++i) y[i] = yt[i * 256];
    const u32x2 dd = ((const u32x2*)dsk)[tid];
    const u32 keyv = ((const u32*)(ws + WS_SEL))[lane & 0];
    asm volatile("" :: "v"(keyv));
    WAITV(); STAMP(2, 1);
    const f32x2 dv[2] = {{lo16(dd[0]), hi16(dd[0])}, {lo16(dd[1]), hi16(dd[1])}};
    u32x2 o[8];
    u32 slo = 0x00003f80u;
    asm volatile("" : "+s"(slo));
#pragma unroll
    for (int r = 0; r < 8; ++r) {
      const float m = rdl(st[0], r), rs = rdl(st[1], r);
#pragma unroll
      for (int j = 0; j < 2; ++j) {
        const f32x2 xf = {lo16(v[r][j]), hi16(v[r][j])};
        const f32x2 df = xf - m;
        u32 t = pkbf(df[0], df[1]);
        const f32x2 nf0 = f32x2{lo16(t), hi16(t)} * rs;
        t = pkbf(nf0[0], nf0[1]);
        const f32x2 nf = {lo16(t), hi16(t)};
        const f32x2 sf = nf * dv[j];
        t = pkbf(sf[0], sf[1]);
        const float ya = (r & 1) ? hi16(y[2 * j][r >> 1]) : lo16(y[2 * j][r >> 1]);
        const float yb = (r & 1) ? hi16(y[2 * j + 1][r >> 1]) : lo16(y[2 * j + 1][r >> 1]);
        t = pkbf(dot2(t, slo, ya), dot2(t, 0x3f800000u, yb));
        t = pkbf(dot2(t, slo, xf[0]), dot2(t, 0x3f800000u, xf[1]));
        o[r][j] = pkbf(dot2(t, slo, nf[0]), dot2(t, 0x3f800000u, nf[1]));
      }
    }
    const int skip = 2047 - (int)(__builtin_amdgcn_readfirstlane(keyv) & 0xffffu) - blk * 8;
    const auto outr = __builtin_amdgcn_make_buffer_rsrc(out, 0, L * D * 2, 0x00020000);
#pragma unroll
    for (int r = 0; r < 8; ++r) {
      asm volatile("" : "+v"(o[r]));
      if (r != skip) st8(outr, (blk * 8 + r) * (D * 2) + tid * 8, o[r]);
    }
    STAMP(2, 2); WAITV(); STAMP(2, 3);
  } else {
    const int cb = blockIdx.x - 256;
    const int cq = tid & 3, pg = tid >> 2;
    const f32x4* pp = (const f32x4*)(ws + WS_PART) + cb * 4 + cq;
    const u32 keyv = ((const u32*)(ws + WS_SEL))[lane & 0];
    f32x4 a0 = pp[pg * 256], a1 = pp[(pg + 64) * 256], a2 = pp[(pg + 128) * 256], a3 = pp[(192 + (pg & 31)) * 256];
    const int d = cb * 16 + (tid & 15);
    const u16 h_d = dsk[d];
    const u32 key = __builtin_amdgcn_readfirstlane(keyv);
    const int idx = 2047 - (int)(key & 0xffffu);
    const u16 h_xn = ((const u16*)(ws + WS_CAND))[(idx >> 3) * 1024 + d];
    const u16 h_y = ((const u16*)(ws + WS_XT))[((idx >> 3) * 1024 + (d & 3) * 256 + (d >> 2)) * 8 + (idx & 7)];
    const u16 h_x = x[idx * D + d];
    if (pg >= 32) a3 = f32x4{0.f, 0.f, 0.f, 0.f};
    f32x4 a = (a0 + a1) + (a2 + a3);
    float t = sw16(sw32(a[0], a[2]), sw32(a[1], a[3]));
    t += dppf<DPP_ROR4>(t); t += dppf<DPP_ROR8>(t);
    if ((lane & 15) < 4) red[(tid >> 6) * 16 + (lane & 3) * 4 + (lane >> 4)] = t;
    __syncthreads();
    if (tid < 16) {
      const float y = (red[tid] + red[16 + tid]) + (red[32 + tid] + red[48 + tid]);
      u32 kb16 = key >> 16;
      kb16 = (kb16 & 0x8000u) ? (kb16 & 0x7fffu) : (~kb16 & 0xffffu);
      const float r = __uint_as_float(kb16 << 16);
      const float rs = rbf(__builtin_amdgcn_rcpf(1.f + exp_acc(-r)));
      const float upd = rbf(rbf(y) * rs);
      const float xn = bf2f(h_xn);
      float o = rbf(bf2f(h_y) + rbf(xn * bf2f(h_d)));
      o = rbf(bf2f(h_x) + o);
      o = o + rbf(xn + upd);
      out[idx * D + d] = (u16)(pkbf(o, o) & 0xffffu);
      WAITV(); STAMP(2, 3);
    }
  }
}

extern "C" void kernel_launch(void* const* d_in, const int* in_sizes, int n_in,
                              void* d_out, int out_size, void* d_ws, size_t ws_size,
                              hipStream_t stream) {
  const u16* x = (const u16*)d_in[0];
  char* ws = (char*)d_ws;
  k1<<<768, 256, 0, stream>>>(x, (const u16*)d_in[8], (const u16*)d_in[1], (const u16*)d_in[2], (const u16*)d_in[3],
                              (const u16*)d_in[4], (const u16*)d_in[5], (const u16*)d_in[9], (const u16*)d_in[10], ws);
  k2<<<256, 768, 0, stream>>>((const u16*)d_in[9], (const u16*)d_in[10], (const u16*)d_in[11], ws);
  k3<<<256 + 64, 256, 0, stream>>>(x, (const u16*)d_in[6], ws, (u16*)d_out);
}
```

```cpp
#include <hip/hip_runtime.h>
#include <stdint.h>

typedef unsigned short u16;
typedef unsigned int u32;
typedef __attribute__((ext_vector_type(2))) u32 u32x2;
typedef __attribute__((ext_vector_type(4))) u32 u32x4;
typedef __attribute__((ext_vector_type(2))) float f32x2;
typedef __attribute__((ext_vector_type(4))) float f32x4;
typedef __attribute__((ext_vector_type(16))) float f32x16;
typedef __attribute__((ext_vector_type(8))) __bf16 bf16x8;
typedef __attribute__((ext_vector_type(2))) __bf16 bf16x2;
typedef __attribute__((ext_vector_type(8))) _Float16 f16x8;
typedef __attribute__((ext_vector_type(2))) _Float16 f16x2;

constexpr int L = 2048, D = 1024, F = 3584;

#define DEVI __device__ __forceinline__
DEVI float lo16(u32 u) { return __uint_as_float(u << 16); }
DEVI float hi16(u32 u) { return __uint_as_float(u & 0xffff0000u); }
DEVI float bf2f(u16 v) { return __uint_as_float(((u32)v) << 16); }
DEVI u32 pkbf(float a, float b) { f32x2 v = {a, b}; return __builtin_bit_cast(u32, __builtin_convertvector(v, bf16x2)); }
DEVI u32 pkh(float a, float b) { f32x2 v = {a, b}; return __builtin_bit_cast(u32, __builtin_convertvector(v, f16x2)); }
DEVI float rbf(float a) { return lo16(pkbf(a, a)); }

template <int CTRL> DEVI float dppf(float v) {
  return __uint_as_float(__builtin_amdgcn_update_dpp(0u, __float_as_uint(v), CTRL, 0xf, 0xf, true));
}
template <int CTRL> DEVI u32 dppu(u32 v) { return __builtin_amdgcn_update_dpp(0u, v, CTRL, 0xf, 0xf, true); }
constexpr int DPP_X1 = 0xB1, DPP_X2 = 0x4E, DPP_HMIR = 0x141, DPP_MIR = 0x140, DPP_ROR8 = 0x128, DPP_WSHR1 = 0x138;
DEVI float row_allsum(float v) {
  v += dppf<DPP_X1>(v); v += dppf<DPP_X2>(v); v += dppf<DPP_HMIR>(v); v += dppf<DPP_MIR>(v); return v;
}
DEVI float sw32(float a, float b) {
  auto r = __builtin_amdgcn_permlane32_swap(__float_as_uint(a), __float_as_uint(b), false, false);
  return __uint_as_float(r[0]) + __uint_as_float(r[1]);
}
DEVI float sw16(float a, float b) {
  auto r = __builtin_amdgcn_permlane16_swap(__float_as_uint(a), __float_as_uint(b), false, false);
  return __uint_as_float(r[0]) + __uint_as_float(r[1]);
}
DEVI u32 umax32(u32 a, u32 b) { return a > b ? a : b; }
DEVI u32 wave_umax(u32 v) {
  v = umax32(v, dppu<DPP_X1>(v)); v = umax32(v, dppu<DPP_X2>(v)); v = umax32(v, dppu<DPP_HMIR>(v)); v = umax32(v, dppu<DPP_MIR>(v));
  u32 a = __builtin_amdgcn_readlane(v, 0), b = __builtin_amdgcn_readlane(v, 16), c = __builtin_amdgcn_readlane(v, 32), d = __builtin_amdgcn_readlane(v, 48);
  return umax32(umax32(a, b), umax32(c, d));
}

constexpr int I3E = 127, I3O = 223, I2A = 255, I2B = 319, I2C = 383, I2D = 447, LM = 975, IX = 1016;
constexpr int ZIDX = 24 * 32;
constexpr int rnd_bf16_int(int l) {
  if (l < 256) return l;
  int s = 0; while ((l >> s) >= 256) ++s;
  int q = l >> s, rem = l & ((1 << s) - 1), half = 1 << (s - 1);
  if (rem > half || (rem == half && (q & 1))) ++q;
  return q << s;
}
constexpr int pos_idx(int p) {
  if (p == 0) return 0;
  int row = 0, alpha = 0;
  if (p <= 256) { alpha = ((p - 1) & 7) + 1; row = alpha; }
  else if (p <= 512) { int o = ((p - 258) & 7) >> 1; alpha = 258 + 2 * o; row = 9 + o; }
  else if (p <= 1024) { int i = (p - 516) >> 8; int o = ((p - 516) & 7) >> 2; alpha = 516 + 256 * i + 4 * o; row = 13 + 2 * i + o; }
  else { int i = (p - 1032) >> 8; alpha = 1032 + 256 * i; row = 17 + i; }
  return row * 32 + (p - alpha) / 8;
}
struct Lut { u16 v[1032]; };
constexpr Lut make_lut() {
  Lut t{};
  for (int i = 0; i < 1032; ++i) t.v[i] = ZIDX;
  for (int m = 64; m <= 127; ++m) { t.v[I3E - m] = pos_idx(16 * (m + 1)); t.v[I3O - m] = pos_idx(16 * m + 8); }
  for (int m = 32; m <= 63; ++m) {
    t.v[I2A - m] = pos_idx(16 * (m + 1));
    if (m <= 62) t.v[I2B - m] = pos_idx(16 * (m + 1) + 4);
    t.v[I2C - m] = pos_idx(16 * m + 8);
    t.v[I2D - m] = pos_idx(16 * m + 12);
  }
  for (int l = 0; l <= 517; ++l) t.v[LM - l] = pos_idx(rnd_bf16_int(l));
  t.v[IX] = pos_idx(1024);
  for (int i = 0; i < 1032; ++i) t.v[i] *= 4;
  return t;
}
__device__ const Lut g_lut = make_lut();

constexpr u32 WS_XT = 0;
constexpr u32 RIN_SZ = 56 * 128 + 128;
constexpr u32 WS_RIN = 4u << 20;
constexpr u32 WS_ST = WS_RIN + 64 * RIN_SZ;
constexpr u32 WS_GRAN = WS_ST + 256 * 64;
constexpr int NREP = 32;
constexpr u32 WS_F1 = WS_GRAN + 256 * 4096;
constexpr u32 WS_GEN = WS_F1 + NREP * 2048;

DEVI float dot2(u32 a, u32 b, float c) {
  return __builtin_amdgcn_fdot2_f32_bf16(__builtin_bit_cast(bf16x2, a), __builtin_bit_cast(bf16x2, b), c, false);
}
DEVI f32x2 rot(f32x2 b) { return f32x2{-b[1], b[0]}; }
DEVI f32x2 cmul(f32x2 a, f32x2 b, f32x2 bs) {
  return __builtin_elementwise_fma(f32x2{a[1], a[1]}, bs, f32x2{a[0], a[0]} * b);
}
DEVI f32x2 cexp2(float lg, float rev) {
  float m = __builtin_amdgcn_exp2f(lg);
  return f32x2{m * __builtin_amdgcn_cosf(rev), m * __builtin_amdgcn_sinf(rev)};
}
DEVI float exp_acc(float x) {
  x = __builtin_amdgcn_fmed3f(x, -200.f, 200.f);
  const float t = x * 1.44269502162933349609375f;
  const float e = __builtin_fmaf(x, 1.44269502162933349609375f, -t) + x * 1.925963033500011e-8f;
  return __builtin_amdgcn_exp2f(t) * __builtin_fmaf(e, 0.6931471805599453f, 1.f);
}
#define NTL(T, p) __builtin_nontemporal_load((const T*)(p))
typedef __attribute__((ext_vector_type(4))) short s16x4;
DEVI u32x2 ds_tr(const char* p) {
  typedef __attribute__((address_space(3))) s16x4* lp;
  return __builtin_bit_cast(u32x2, __builtin_amdgcn_ds_read_tr16_b64_v4i16((lp)p));
}
typedef __amdgpu_buffer_rsrc_t rsrc_t;
DEVI void st16(rsrc_t r, u32 off, u32x4 v) { __builtin_amdgcn_raw_buffer_store_b128(v, r, off, 0, 16); }
DEVI void st8(rsrc_t r, u32 off, u32x2 v) { __builtin_amdgcn_raw_buffer_store_b64(v, r, off, 0, 16); }
DEVI void st4(rsrc_t r, u32 off, u32 v) { __builtin_amdgcn_raw_buffer_store_b32(v, r, off, 0, 16); }
DEVI u32x4 ld16(rsrc_t r, u32 off) { return __builtin_amdgcn_raw_buffer_load_b128(r, off, 0, 16); }
DEVI u32x2 ld8(rsrc_t r, u32 off) { return __builtin_amdgcn_raw_buffer_load_b64(r, off, 0, 16); }
DEVI u32 ld4(rsrc_t r, u32 off) { return __builtin_amdgcn_raw_buffer_load_b32(r, off, 0, 16); }
#define DRAIN() asm volatile("s_waitcnt vmcnt(0)" ::: "memory")
#define CBAR() asm volatile("" ::: "memory")
constexpr int SPIN_G = 1 << 16, SPIN_L = 1 << 20;
constexpr u32 OOB = 0x7ffffff0u;
constexpr int PFD = 4;
constexpr int CODE_PF_BYTES = 25600;

DEVI u32x4 make_box(int ce, int w, int rho, int kg, u32 val) {
  int base = 8 * kg - 16 - rho - ce;
  int sh = 22 + base; sh = sh < 0 ? 0 : (sh > 31 ? 31 : sh);
  u32 bits = (((1u << w) - 1u) << 22) >> sh;
  u32x4 o;
#pragma unroll
  for (int p = 0; p < 4; ++p) o[p] = __umul24(__umul24((bits >> (2 * p)) & 3u, 0x8001u) & 0x10001u, val);
  return o;
}
DEVI bf16x8 asbf(u32x4 v) { return __builtin_bit_cast(bf16x8, v); }
DEVI bf16x8 ld2x64(const char* p, int off2) {
  typedef const volatile __attribute__((address_space(3))) u32x2* lp;
  const u32x2 a = *(lp)p, b = *(lp)(p + off2);
  return asbf(u32x4{a[0], a[1], b[0], b[1]});
}
DEVI f32x4 mf16(bf16x8 a, bf16x8 b, f32x4 c) { return __builtin_amdgcn_mfma_f32_16x16x32_bf16(a, b, c, 0, 0, 0); }
DEVI bf16x8 packx(f32x4 a, f32x4 b) { return asbf(u32x4{pkbf(a[0], a[1]), pkbf(a[2], a[3]), pkbf(b[0], b[1]), pkbf(b[2], b[3])}); }
typedef __attribute__((address_space(3))) u32 lu32;
DEVI u32 lds_ld(lu32* p) { return __hip_atomic_load(p, __ATOMIC_RELAXED, __HIP_MEMORY_SCOPE_WORKGROUP); }
DEVI void lds_st(lu32* p, u32 v) { __hip_atomic_store(p, v, __ATOMIC_RELAXED, __HIP_MEMORY_SCOPE_WORKGROUP); }
DEVI u32 lds_wait_ge(lu32* p, u32 v) {
  u32 r = 0;
#pragma unroll 1
  for (int i = 0; i < SPIN_L; ++i) { r = __builtin_amdgcn_readfirstlane(lds_ld(p)); if (r >= v) break; __builtin_amdgcn_s_sleep(1); }
  CBAR();
  return r;
}
DEVI u32 lds_signal(lu32* p) {
  asm volatile("s_waitcnt lgkmcnt(0)" ::: "memory");
  u32 r = 0;
  if ((threadIdx.x & 63) == 0) r = __hip_atomic_fetch_add(p, 1u, __ATOMIC_RELAXED, __HIP_MEMORY_SCOPE_WORKGROUP);
  r = __builtin_amdgcn_readfirstlane(r);
  CBAR();
  return r;
}
DEVI u32 mktag(u32 gen) { return gen + 1u == 0u ? 1u : gen + 1u; }
constexpr int KG_ROW = 528, KG_WAVE = 32 * KG_ROW;
constexpr int LN_ROW = 2048 + 64;
constexpr int U_STRIDE = 4640, YW_STRIDE = 6592;
constexpr int KT0 = 0;
constexpr int U0 = 32768, XR0 = U0 + 4 * U_STRIDE;
constexpr int LNI = XR0;
constexpr int MISC0 = XR0 + 4 * U_STRIDE;
constexpr int SCR = MISC0 + 10240;
constexpr int YW0 = SCR, YP0 = SCR + 4 * YW_STRIDE;
constexpr int LDS_TOTAL = SCR + 4 * KG_WAVE;
static_assert(YP0 % 16 == 0 && YP0 + 32768 <= LDS_TOTAL && 8 * LN_ROW <= 4 * U_STRIDE, "lds");
enum { FL_PDONE = 0, FL_MOE, FL_STAGED, FL_LN, FL_KG, FL_RK, FL_INV, FL_IDX, FL_TAG, FL_KEY, FL_MSD  , FL_XV = FL_MSD + 2, FL_H, FL_D, FL_H0 };

DEVI void final_rows(const char* smem, int T, bool use_w, u32x2 fd, int idx, int B, rsrc_t wsr, u32 tag, u32x2 (&o)[4], lu32* ready) {
  const int fo = ((T >> 1) + (T >> 4) + 3) * 16 + (T & 1) * 8;
  u32 slo = 0x00003f80u;
  asm volatile("" : "+s"(slo));
  float px[4][4], pn[4][4], pt[4][4];
#pragma unroll
  for (int k = 0; k < 4; ++k) {
    const u32x2 nn = *(const u32x2*)(smem + U0 + k * U_STRIDE + fo), xx = *(const u32x2*)(smem + XR0 + k * U_STRIDE + fo);
    const float dv = (k & 1) ? hi16(fd[k >> 1]) : lo16(fd[k >> 1]);
#pragma unroll
    for (int j = 0; j < 2; ++j) {
      const f32x2 nf = {lo16(nn[j]), hi16(nn[j])};
      const f32x2 sf = nf * dv;
      const u32 t = pkbf(sf[0], sf[1]);
      pn[k][2 * j] = nf[0]; pn[k][2 * j + 1] = nf[1];
      pt[k][2 * j] = lo16(t); pt[k][2 * j + 1] = hi16(t);
      px[k][2 * j] = lo16(xx[j]); px[k][2 * j + 1] = hi16(xx[j]);
    }
  }
  __builtin_amdgcn_sched_barrier(0);
  lds_wait_ge(ready, 8);
  f32x4 yv[4];
#pragma unroll
  for (int k = 0; k < 4; ++k) yv[k] = *(const f32x4*)(smem + YP0 + k * 8192 + (T ^ ((T >> 3) & 7)) * 16);
  if (use_w) {
#pragma unroll
    for (int k = 0; k < 4; ++k) yv[k] += *(const f32x4*)(smem + YW0 + k * YW_STRIDE + ((T - 128) + ((T - 128) >> 4)) * 16);
  }
  if (T == (idx >> 2)) {
    const int i = idx & 3;
    float yy[4];
#pragma unroll
    for (int k = 0; k < 4; ++k) yy[k] = i == 0 ? yv[k][0] : i == 1 ? yv[k][1] : i == 2 ? yv[k][2] : yv[k][3];
    st16(wsr, WS_RIN + (B >> 2) * RIN_SZ + 56 * 128 + (B & 3) * 16, u32x4{pkbf(yy[0], yy[1]), tag, pkbf(yy[2], yy[3]), tag});
  }
#pragma unroll
  for (int j = 0; j < 2; ++j) {
    float r[4][2];
#pragma unroll
    for (int k = 0; k < 4; ++k) {
      u32 t = pkbf(yv[k][2 * j], yv[k][2 * j + 1]);
      t = pkbf(dot2(t, slo, pt[k][2 * j]), dot2(t, 0x3f800000u, pt[k][2 * j + 1]));
      t = pkbf(dot2(t, slo, px[k][2 * j]), dot2(t, 0x3f800000u, px[k][2 * j + 1]));
      r[k][0] = dot2(t, slo, pn[k][2 * j]); r[k][1] = dot2(t, 0x3f800000u, pn[k][2 * j + 1]);
    }
    o[2 * j] = u32x2{pkbf(r[0][0], r[1][0]), pkbf(r[2][0], r[3][0])};
    o[2 * j + 1] = u32x2{pkbf(r[0][1], r[1][1]), pkbf(r[2][1], r[3][1])};
  }
}
DEVI void store_row(rsrc_t outr, int t, int idx, int B, u32x2 v) {
  __builtin_amdgcn_raw_buffer_store_b64(v, outr, t != idx ? t * (D * 2) + B * 8 : OOB, 0, 0);
}

__global__ __launch_bounds__(768, 3) void fused(const u16* __restrict__ x, const u16* __restrict__ wr,
                                                const u16* __restrict__ lar, const u16* __restrict__ aim,
                                                const u16* __restrict__ cre, const u16* __restrict__ cim,
                                                const u16* __restrict__ ldt, const u16* __restrict__ dsk,
                                                const u16* __restrict__ wg, const u16* __restrict__ wu,
                                                const u16* __restrict__ wd, char* __restrict__ ws, u16* __restrict__ out) {
  __shared__ __attribute__((aligned(16))) char smem[LDS_TOTAL];
  const int tid = threadIdx.x, lane = tid & 63, wv = __builtin_amdgcn_readfirstlane(tid >> 6);
  const int blk = blockIdx.x, bx = blk & 7, bk = blk >> 3;
  const rsrc_t wsr = __builtin_amdgcn_make_buffer_rsrc(ws, 0, WS_GEN + 64, 0x00020000);
  lu32* const flg = (lu32*)(smem + MISC0);
  const u32 rep = (u32)(blk & (NREP - 1));
  const int B = bx * 32 + bk;
  u32x4 v[4], w[4];
  u32 gen, h_ldt, cpf;
  const int lr = tid >> 5, lc = tid & 31;
  const u32x4* xp = (const u32x4*)(x + (blk * 8 + lr) * D) + lc;
  if (wv >= 4) {
   if (wv >= 8) {
    const u32x4 inv = {0u, 0u, 0u, 0u};
    const u32 ro = WS_RIN + (blk >> 2) * RIN_SZ + (blk & 3) * (RIN_SZ / 4);
    st16(wsr, tid - 512 < RIN_SZ / 64 ? ro + (tid - 512) * 16 : OOB, inv);
    st16(wsr, WS_GRAN + blk * 4096 + (tid - 512) * 16, inv);
    gen = ld4(wsr, WS_GEN);
    if (tid - 512 < CODE_PF_BYTES / 128)
      cpf = *(const __attribute__((address_space(1))) u32*)(__builtin_amdgcn_s_getpc() + (tid - 512) * 128);
   } else {
    const int d = 4 * B + (wv & 3);
    v[0][0] = NTL(u32, (const u32*)lar + d * 64 + lane); v[0][1] = NTL(u32, (const u32*)aim + d * 64 + lane);
    v[0][2] = NTL(u32, (const u32*)cre + d * 64 + lane); v[0][3] = NTL(u32, (const u32*)cim + d * 64 + lane);
    h_ldt = ldt[d];
#pragma unroll
    for (int i = 0; i < 2; ++i) {
      w[i] = *(const u32x4*)(g_lut.v + (i * 64 + lane) * 8);
      const u32x2 t = *(const u32x2*)(g_lut.v + (i * 64 + lane) * 8 + 8);
      w[2][2 * i] = t[0]; w[2][2 * i + 1] = t[1];
    }
   }
  } else {
#pragma unroll
    for (int i = 0; i < 4; ++i) v[i] = NTL(u32x4, xp + i * 32);
    gen = ld4(wsr, WS_GEN);
#pragma unroll
    for (int i = 0; i < 4; ++i) w[i] = ((const u32x4*)wr)[i * 32 + lc];
    if (tid < 32) lds_st(flg + tid, 0u);
  }
  asm volatile("s_waitcnt lgkmcnt(0)" ::: "memory");
  __builtin_amdgcn_s_barrier();
  CBAR();
  if (wv >= 8) {
    u32x4 g[8], u[8];
    u32x4 dn[8];
    const int mt = tid - 512;
    const bool moe = bk < 28;
    const int lg = 8 * (bk >> 2) + bx, kq = bk & 3;
    const int rg = mt >> 3, q = mt & 7;
    const u16* pg = wg + (size_t)(kq * 256 + 8 * rg) * F + 64 * lg + 8 * q;
    const u16* pu = wu + (size_t)(kq * 256 + 8 * rg) * F + 64 * lg + 8 * q;
    const u16* pd = wd + (size_t)(64 * lg + 8 * (mt >> 5)) * D + 256 * kq + 8 * (mt & 31);
    DRAIN();
    asm volatile("" :: "v"(cpf));
    if (lane == 0) lds_st(flg + FL_TAG, mktag(gen));
    lds_signal(flg + FL_INV);
    if (moe) {
      lds_wait_ge(flg + FL_RK, 4);
#pragma unroll
      for (int i = 0; i < 8; ++i) {
        g[i] = NTL(u32x4, pg + i * F); u[i] = NTL(u32x4, pu + i * F);
        __builtin_amdgcn_sched_barrier(0);
        __builtin_amdgcn_s_sleep(6);
        __builtin_amdgcn_sched_barrier(0);
      }
    }
    __builtin_amdgcn_s_setprio(3);
    const u32 tag = mktag(__builtin_amdgcn_readfirstlane(gen));
    const int idx = ((int)lds_wait_ge(flg + FL_IDX, 1) - 1) & (L - 1);
    const u32 key = __builtin_amdgcn_readfirstlane(lds_ld(flg + FL_KEY));
    if (blk == 0 && mt == 0) st4(wsr, WS_GEN, tag);
    if (moe) {
#pragma unroll
      for (int j = 0; j < 8; ++j) dn[j] = NTL(u32x4, pd + j * D);
      __builtin_amdgcn_sched_barrier(0);
      lds_wait_ge(flg + FL_XV, 1);
      const u32x4 xv = *(const u32x4*)(smem + MISC0 + 2560 + rg * 16);
      float s[16];
#pragma unroll
      for (int i = 0; i < 16; ++i) s[i] = 0.f;
#pragma unroll
      for (int p = 0; p < 4; ++p) {
#pragma unroll
        for (int j = 0; j < 4; ++j) {
          s[2 * j] = dot2(__builtin_amdgcn_perm(g[2 * p + 1][j], g[2 * p][j], 0x05040100u), xv[p], s[2 * j]);
          s[2 * j + 1] = dot2(__builtin_amdgcn_perm(g[2 * p + 1][j], g[2 * p][j], 0x07060302u), xv[p], s[2 * j + 1]);
          s[8 + 2 * j] = dot2(__builtin_amdgcn_perm(u[2 * p + 1][j], u[2 * p][j], 0x05040100u), xv[p], s[8 + 2 * j]);
          s[8 + 2 * j + 1] = dot2(__builtin_amdgcn_perm(u[2 * p + 1][j], u[2 * p][j], 0x07060302u), xv[p], s[8 + 2 * j + 1]);
        }
      }
#pragma unroll
      for (int i = 0; i < 8; ++i) s[i] = sw32(s[i], s[i + 8]);
#pragma unroll
      for (int i = 0; i < 4; ++i) { float t = sw16(s[i], s[i + 4]); s[i] = t + dppf<DPP_ROR8>(t); }
      float* red = (float*)(smem + MISC0 + 256);
      if ((lane & 8) == 0) *(f32x4*)(red + (wv - 8) * 128 + (lane >> 5) * 64 + 8 * q + ((lane >> 4) & 1) * 4) = f32x4{s[0], s[1], s[2], s[3]};
      lds_signal(flg + FL_MOE);
      lds_wait_ge(flg + FL_MOE, 4);
      {
        const f32x2* rp = (const f32x2*)red + lane;
        const f32x2 v = (rp[0] + rp[64]) + (rp[128] + rp[192]);
        st16(wsr, WS_GRAN + ((blk & ~24) | ((wv - 8) << 3)) * 4096 + kq * 1024 + lane * 16, u32x4{__float_as_uint(v[0]), tag, __float_as_uint(v[1]), tag});
      }
      u32x4 ga = {0u, 0u, 0u, 0u};
#pragma unroll 1
      for (int it = 0; it < SPIN_G; ++it) {
        CBAR();
        if (ga[1] != tag || ga[3] != tag) ga = ld16(wsr, WS_GRAN + blk * 4096 + (wv - 8) * 1024 + lane * 16);
        if (__builtin_amdgcn_ballot_w64(ga[1] == tag && ga[3] == tag) == ~0ull) break;
        __builtin_amdgcn_s_sleep(1);
      }
      u32* red3 = (u32*)(smem + MISC0 + 3072);
      *(u32x2*)(red3 + (wv - 8) * 128 + lane * 2) = u32x2{ga[0], ga[2]};
      lds_signal(flg + FL_H);
      lds_wait_ge(flg + FL_H, 4);
      {
        const float* rp = (const float*)red3 + lane;
        const float gs = rbf((rp[0] + rp[256]) + (rp[128] + rp[384])), us = rbf((rp[64] + rp[320]) + (rp[192] + rp[448]));
        const float sg = rbf(gs * __builtin_amdgcn_rcpf(1.f + exp_acc(-gs)));
        *(u16*)(smem + MISC0 + 5120 + (wv - 8) * 128 + lane * 2) = (u16)pkbf(sg * us, 0.f);
      }
      CBAR();
      const u32x4 hp = *(const u32x4*)(smem + MISC0 + 5120 + (wv - 8) * 128 + (mt >> 5) * 16);
      float o[8];
#pragma unroll
      for (int i = 0; i < 8; ++i) o[i] = 0.f;
#pragma unroll
      for (int p = 0; p < 4; ++p) {
#pragma unroll
        for (int j = 0; j < 4; ++j) {
          o[2 * j] = dot2(__builtin_amdgcn_perm(dn[2 * p + 1][j], dn[2 * p][j], 0x05040100u), hp[p], o[2 * j]);
          o[2 * j + 1] = dot2(__builtin_amdgcn_perm(dn[2 * p + 1][j], dn[2 * p][j], 0x07060302u), hp[p], o[2 * j + 1]);
        }
      }
      float* red4 = (float*)(smem + MISC0 + 5632);
      *(f32x4*)(red4 + (wv - 8) * 256 + (lane & 31) * 8 + (lane >> 5) * 4) = f32x4{sw32(o[0], o[4]), sw32(o[1], o[5]), sw32(o[2], o[6]), sw32(o[3], o[7])};
      lds_signal(flg + FL_D);
      lds_wait_ge(flg + FL_D, 4);
      if (wv < 10) {
        const f32x2* rp = (const f32x2*)red4 + mt;
        const f32x2 yp = (rp[0] + rp[128]) + (rp[256] + rp[384]);
        st16(wsr, WS_RIN + (16 * kq + (mt >> 3)) * RIN_SZ + lg * 128 + (mt & 7) * 16, u32x4{__float_as_uint(yp[0]), tag, __float_as_uint(yp[1]), tag});
      }
    }
    if (blk < 64 && wv == 8) {
      const int d = blk * 16 + (lane & 7) * 2 + (lane >> 5);
      const u16 h_d = dsk[d], h_x = x[idx * D + d];
      u32x4 p[7];
      u32x2 yg = {0u, 0u};
#pragma unroll
      for (int k = 0; k < 7; ++k) p[k] = u32x4{0u, 0u, 0u, 0u};
#pragma unroll 1
      for (int it = 0; it < SPIN_G; ++it) {
        CBAR();
        bool ok = true;
#pragma unroll
        for (int k = 0; k < 7; ++k) if (p[k][1] != tag || p[k][3] != tag) p[k] = ld16(wsr, WS_RIN + blk * RIN_SZ + k * 1024 + lane * 16);
        if (yg[1] != tag) yg = ld8(wsr, WS_RIN + blk * RIN_SZ + 56 * 128 + (lane & 7) * 8);
#pragma unroll
        for (int k = 0; k < 7; ++k) ok = ok && p[k][1] == tag && p[k][3] == tag;
        if (__builtin_amdgcn_ballot_w64(ok && yg[1] == tag) == ~0ull) break;
        __builtin_amdgcn_s_sleep(1);
      }
      f32x2 a = {0.f, 0.f};
#pragma unroll
      for (int k = 0; k < 7; ++k) a += f32x2{__uint_as_float(p[k][0]), __uint_as_float(p[k][2])};
      float y = sw32(a[0], a[1]);
      y = sw16(y, y);
      y += dppf<DPP_ROR8>(y);
      u32 kb16 = key >> 16;
      kb16 = (kb16 & 0x8000u) ? (kb16 & 0x7fffu) : (~kb16 & 0xffffu);
      const float rs = rbf(__builtin_amdgcn_rcpf(1.f + exp_acc(-__uint_as_float(kb16 << 16))));
      const float upd = rbf(rbf(y) * rs);
      const float xn = rbf(rbf(bf2f(h_x) - __uint_as_float(lds_ld(flg + FL_MSD))) * __uint_as_float(lds_ld(flg + FL_MSD + 1)));
      float o = rbf((lane >= 32 ? hi16(yg[0]) : lo16(yg[0])) + rbf(xn * bf2f(h_d)));
      o = rbf(bf2f(h_x) + o);
      o = o + rbf(xn + upd);
      if ((lane & 24) == 0) out[idx * D + d] = (u16)(pkbf(o, o) & 0xffffu);
    }
  } else {
    const int c = wv & 3;
    const u32x2 fd = *(const u32x2*)(dsk + 4 * B);
    const rsrc_t outr = __builtin_amdgcn_make_buffer_rsrc(out, 0, L * D * 2, 0x00020000);
    if (wv >= 4) {
    u32x4 bxs[6], bxx;
      asm volatile("" : "+v"(h_ldt));
      const u32 h_lar = v[0][0], h_aim = v[0][1], h_cr = v[0][2], h_ci = v[0][3];
      const u32x4 lut[2] = {w[0], w[1]};
      const u32x2 luh[2] = {{w[2][0], w[2][1]}, {w[2][2], w[2][3]}};
      {
        const int rho = lane & 15, kg = lane >> 4;
        bxs[0] = make_box(-13, 5, rho, kg, 0x3f80u); bxs[1] = make_box(-16, 3, rho, kg, 0x3f80u); bxs[2] = make_box(-5, 5, rho, kg, 0x3f80u);
        bxs[3] = make_box(-8, 3, rho, kg, 0x3f80u); bxs[4] = make_box(-15, 9, rho, kg, 0x3f80u); bxs[5] = make_box(-6, 7, rho, kg, 0x3f80u);
        bxx = make_box(-15, 2, rho, kg, 1u);
      }
      __builtin_amdgcn_sched_barrier(0);
#pragma unroll
      for (int i = 0; i < 6; ++i) asm volatile("" : "+v"(bxs[i]));
      asm volatile("" : "+v"(bxx));
      __builtin_amdgcn_sched_barrier(0);
      const float dtv = rbf(exp_acc(lo16(h_ldt)));
      f32x2 e1[2], e8[2], e256[2], g0[2];
      u32 mx = 0;
#pragma unroll
      for (int k = 0; k < 2; ++k) {
        const float zr = -rbf(exp_acc(k ? hi16(h_lar) : lo16(h_lar))) * dtv;
        const float zi = (k ? hi16(h_aim) : lo16(h_aim)) * dtv;
        const float lg = zr * 1.44269504088896341f;
        const float c_hi = 0.15915494309189535f, c_lo = 6.4206382e-9f;
        const float p = zi * c_hi;
        const float tl = __builtin_fmaf(zi, c_hi, -p) + zi * c_lo;
        const float th = p - __builtin_rintf(p);
        e1[k] = cexp2(lg, th + tl);
        e8[k] = cexp2(lg * 8.f, __builtin_amdgcn_fractf(th * 8.f) + tl * 8.f);
        e256[k] = cexp2(lg * 256.f, __builtin_amdgcn_fractf(th * 256.f) + tl * 256.f);
        g0[k] = f32x2{(k ? hi16(h_cr) : lo16(h_cr)), -(k ? hi16(h_ci) : lo16(h_ci))} * (2.f * dtv);
        mx = umax32(mx, umax32(__float_as_uint(g0[k][0]) & 0x7fffffffu, __float_as_uint(g0[k][1]) & 0x7fffffffu));
      }
      mx = wave_umax(mx);
      int ex = (int)(mx >> 23); ex = ex < 1 ? 1 : (ex > 253 ? 253 : ex);
      const float sinv = __uint_as_float((u32)(254 - ex) << 23);
      char* base = smem + SCR + c * KG_WAVE;
      char* wp = base + lane * 8;
#define WROW(rw, cc) *(u32x2*)(wp + (rw) * KG_ROW) = u32x2{pkh((cc)[0][0], (cc)[0][1]), pkh((cc)[1][0], (cc)[1][1])}
#define CMUL2(o, a, b, rb) do { o[0] = cmul(a[0], b[0], rb[0]); o[1] = cmul(a[1], b[1], rb[1]); } while (0)
      *(u32x2*)wp = u32x2{0x00003c00u, 0x00003c00u};
      {
        const f32x2 r1[2] = {rot(e1[0]), rot(e1[1])}, r256[2] = {rot(e256[0]), rot(e256[1])};
        f32x2 p[2], t[2];
        WROW(1, e1); WROW(8, e8);
        CMUL2(p, e1, e1, r1); WROW(2, p); CMUL2(t, p, e256, r256); WROW(9, t);
        CMUL2(p, p, e1, r1); WROW(3, p);
        CMUL2(p, p, e1, r1); WROW(4, p); CMUL2(t, p, e256, r256); WROW(10, t); CMUL2(t, t, e256, r256); WROW(13, t); CMUL2(t, t, e256, r256); WROW(15, t);
        CMUL2(p, p, e1, r1); WROW(5, p);
        CMUL2(p, p, e1, r1); WROW(6, p); CMUL2(t, p, e256, r256); WROW(11, t);
        CMUL2(p, p, e1, r1); WROW(7, p);
        CMUL2(t, e8, e256, r256); WROW(12, t); CMUL2(t, t, e256, r256); WROW(14, t); CMUL2(t, t, e256, r256); WROW(16, t);
        CMUL2(t, t, e256, r256); WROW(17, t); CMUL2(t, t, e256, r256); WROW(18, t); CMUL2(t, t, e256, r256); WROW(19, t); CMUL2(t, t, e256, r256); WROW(20, t);
      }
      const char* rp = base + (lane & 31) * KG_ROW + (lane >> 5) * 16;
      f16x8 af[16];
      CBAR();
#pragma unroll
      for (int s = 0; s < 16; ++s) af[s] = *(const f16x8*)(rp + s * 32);
      CBAR();
      {
        const f32x2 c8[2] = {{e8[0][0], -e8[0][1]}, {e8[1][0], -e8[1][1]}}, r8[2] = {rot(c8[0]), rot(c8[1])};
        f32x2 c16[2], ga[2] = {g0[0] * sinv, g0[1] * sinv}, gb[2];
        CMUL2(c16, c8, c8, r8); CMUL2(gb, ga, c8, r8);
        const f32x2 r16[2] = {rot(c16[0]), rot(c16[1])};
#pragma unroll 4
        for (int k = 0; k < 16; ++k) { WROW(2 * k, ga); WROW(2 * k + 1, gb); CMUL2(ga, ga, c16, r16); CMUL2(gb, gb, c16, r16); }
      }
      CBAR();
      f32x16 acc = {};
#pragma unroll
      for (int s = 0; s < 16; ++s) acc = __builtin_amdgcn_mfma_f32_32x32x16_f16(af[s], *(const f16x8*)(rp + s * 32), acc, 0, 0, 0);
      CBAR();
      float* kp = (float*)base + (lane >> 5) * 128 + (lane & 31);
#pragma unroll
      for (int r = 0; r < 12; ++r) kp[((r & 3) + 8 * (r >> 2)) * 32] = __builtin_amdgcn_ldexpf(acc[r], ex - 127);
      if (lane == 0) kp[ZIDX] = 0.f;
      CBAR();
#pragma unroll
      for (int i = 0; i < 2; ++i) {
        u32 e[6];
#pragma unroll
        for (int j = 0; j < 6; ++j) {
          const u32 lw = j < 4 ? lut[i][j] : luh[i][j - 4];
          e[j] = pkbf(*(const float*)(base + (lw & 0xffffu)), j < 5 ? *(const float*)(base + (lw >> 16)) : 0.f);
        }
        u32 o[5];
#pragma unroll
        for (int j = 0; j < 5; ++j) o[j] = __builtin_amdgcn_alignbit(e[j + 1], e[j], 16);
        char* tw = smem + KT0 + c * 8192 + (i * 64 + lane) * 64;
#pragma unroll
        for (int j = 0; j < 4; ++j) *(u32x4*)(tw + j * 16) = u32x4{e[j], e[j + 1], o[j], o[j + 1]};
      }
      lds_signal(flg + FL_KG);
      lds_wait_ge(flg + FL_KG, 4);
      if (lane < 8) *(float*)(smem + YW0 + c * YW_STRIDE + lane * 4) = 0.f;
    {
    lds_wait_ge(flg + FL_IDX, 1);
    __builtin_amdgcn_s_setprio(3);
    const int stb = tid >> 2, sc = tid & 3;
    u32x4 xr[2]; f32x4 sm[2][4];
#pragma unroll
    for (int k = 0; k < 2; ++k) {
      xr[k] = ld16(wsr, WS_XT + (stb + 128 * k) * 16384 + B * 64 + sc * 16);
#pragma unroll
      for (int j = 0; j < 4; ++j) sm[k][j] = __builtin_bit_cast(f32x4, ld16(wsr, WS_ST + j * 4096 + (stb + 128 * k) * 16));
    }
    char* sdst = smem + U0 + sc * U_STRIDE + (stb + (stb >> 3) + 3) * 16;
#pragma unroll
    for (int k = 0; k < 2; ++k) {
      f32x2 df[4]; u32x4 z;
#pragma unroll
      for (int j = 0; j < 4; ++j) df[j] = f32x2{lo16(xr[k][j]), hi16(xr[k][j])} - f32x2{sm[k][j >> 1][2 * (j & 1)], sm[k][j >> 1][2 * (j & 1) + 1]};
#pragma unroll
      for (int j = 0; j < 4; ++j) z[j] = pkbf(df[j][0], df[j][1]);
#pragma unroll
      for (int j = 0; j < 4; ++j) df[j] = f32x2{lo16(z[j]), hi16(z[j])} * f32x2{sm[k][2 + (j >> 1)][2 * (j & 1)], sm[k][2 + (j >> 1)][2 * (j & 1) + 1]};
#pragma unroll
      for (int j = 0; j < 4; ++j) z[j] = pkbf(df[j][0], df[j][1]);
      *(u32x4*)(sdst + k * 144 * 16) = z;
      *(u32x4*)(sdst + (XR0 - U0) + k * 144 * 16) = xr[k];
    }
    lds_signal(flg + FL_STAGED);
    }
    {
    __builtin_amdgcn_s_setprio(0);
    lds_wait_ge(flg + FL_KG, 4);
    lds_wait_ge(flg + FL_STAGED, 8);
    const char* ub = smem + U0 + c * U_STRIDE;
    const char* kb = smem + KT0 + c * 8192;
      __builtin_amdgcn_s_setprio(3);
      const int rho = lane & 15, kg = lane >> 4;
      bf16x8 uf[6];
      {
        const int pc = 2 * rho - 2 + kg;
        const char* up = ub + (pc + (pc >> 3) + 3) * 16;
#pragma unroll
        for (int b = 0; b < 6; ++b) uf[b] = asbf(*(const u32x4*)(up + b * 576));
      }
      const char* kp = kb + (4 * kg - rho) * 8;
      const f32x4 z = {};
      constexpr int IC[6] = {I2A - 32, I2B - 32, I2C - 32, I2D - 32, I3E - 64, I3O - 64};
      float* yq[4];
#pragma unroll
      for (int i = 0; i < 4; ++i) { const int u = 64 * kg + 16 * i + rho + 5; yq[i] = (float*)(smem + YW0 + c * YW_STRIDE) + u + 4 * (u >> 6); }
      const int pcx = 2 * rho + kg;
      bf16x8 Bf0[4];
      {
        const u32 kx = *(const u16*)(kb + IX * 8);
        int pm = pcx - 32; pm = pm < -2 ? -2 : pm;
        const u32x4 um = *(const u32x4*)(ub + (pm + (pm >> 3) + 3) * 16);
        f32x4 X[8];
#pragma unroll
        for (int cl = 0; cl < 4; ++cl) { X[2 * cl] = mf16(uf[0], asbf(bxs[cl]), z); X[2 * cl + 1] = mf16(uf[1], asbf(bxs[cl]), z); }
#pragma unroll
        for (int p = 0; p < 4; ++p) bxx[p] = __umul24(bxx[p], kx);
        f32x4 Y0 = z, Y1 = mf16(asbf(um), asbf(bxx), z);
#pragma unroll
        for (int cl = 0; cl < 4; ++cl) {
          Bf0[cl] = packx(X[2 * cl], X[2 * cl + 1]);
          Y0 = mf16(ld2x64(kp + IC[cl] * 8, 128), Bf0[cl], Y0);
          Y1 = mf16(ld2x64(kp + (IC[cl] - 16) * 8, 128), Bf0[cl], Y1);
        }
#pragma unroll
        for (int i = 0; i < 4; ++i) { yq[i][0] = Y0[i]; yq[i][272] = Y1[i]; }
        lds_signal(flg + FL_H0);
      }
      __builtin_amdgcn_s_setprio(1);
      {
        f32x4 Y[4], X[2][4];
        const char* up = ub + (pcx + (pcx >> 3) + 3) * 16;
#pragma unroll
        for (int b = 0; b < 4; ++b) X[0][b] = mf16(uf[2 + b], asbf(bxs[0]), z);
#pragma unroll
        for (int b = 0; b < 4; ++b) Y[b] = mf16(asbf(*(const u32x4*)(up + b * 576)), asbf(bxx), z);
#pragma unroll
        for (int cl = 0; cl < 6; ++cl) {
          bf16x8 Af[4];
#pragma unroll
          for (int i = 0; i < 4; ++i) Af[i] = ld2x64(kp + (IC[cl] - 16 * i) * 8, 128);
          if (cl < 5) {
#pragma unroll
            for (int b = 0; b < 4; ++b) X[(cl + 1) & 1][b] = mf16(uf[(cl + 1 < 4 ? 2 : 0) + b], asbf(bxs[cl + 1]), z);
          }
          if (cl < 4) { Y[0] = mf16(Af[2], Bf0[cl], Y[0]); Y[1] = mf16(Af[3], Bf0[cl], Y[1]); }
          const bf16x8 Ba = packx(X[cl & 1][0], X[cl & 1][1]), Bb = packx(X[cl & 1][2], X[cl & 1][3]);
#pragma unroll
          for (int i = 0; i < 4; ++i) Y[i] = mf16(Af[i], Ba, Y[i]);
          Y[2] = mf16(Af[0], Bb, Y[2]); Y[3] = mf16(Af[1], Bb, Y[3]);
        }
#pragma unroll
        for (int i = 0; i < 4; ++i)
#pragma unroll
          for (int T = 0; T < 4; ++T) yq[i][(2 + T) * 272] = Y[T][i];
      }
      lds_signal(flg + FL_PDONE);
      __builtin_amdgcn_s_setprio(3);
      const int idx = ((int)lds_ld(flg + FL_IDX) - 1) & (L - 1);
      const u32 tag = lds_ld(flg + FL_TAG);
      u32x2 oh[4];
      final_rows(smem, tid, true, fd, idx, B, wsr, tag, oh, flg + FL_PDONE);
#pragma unroll
      for (int r = 0; r < 4; ++r) {
        if (r == 2) DRAIN();
        store_row(outr, 4 * tid + r, idx, B, oh[r]);
      }
    }

    } else {
    __builtin_amdgcn_s_setprio(3);
    char* lw = smem + LNI + lr * LN_ROW + lc * 16;
#pragma unroll
    for (int i = 0; i < 4; ++i) *(u32x4*)(lw + i * 512) = v[i];
    asm volatile("s_waitcnt lgkmcnt(0)" ::: "memory");
    if (lane == 0) __hip_atomic_fetch_add(flg + FL_LN, 1u, __ATOMIC_RELAXED, __HIP_MEMORY_SCOPE_WORKGROUP);
    float s1[2] = {0.f, 0.f}, s2[2] = {0.f, 0.f};
#pragma unroll
    for (int i = 0; i < 4; ++i)
#pragma unroll
      for (int j = 0; j < 4; ++j) { s1[j & 1] = dot2(v[i][j], 0x3f803f80u, s1[j & 1]); s2[j & 1] = dot2(v[i][j], v[i][j], s2[j & 1]); }
    asm volatile("" : "+v"(s1[0]), "+v"(s1[1]), "+v"(s2[0]), "+v"(s2[1]));
    lds_wait_ge(flg + FL_LN, 4);
    {
      const char* tp = smem + LNI + ((lane >> 2) & 3) * LN_ROW + (wv * 256 + (lane >> 4) * 16 + (lane & 3) * 4) * 2;
      const u32 xo = WS_XT + (blk * 1024 + wv * 256 + lane) * 16;
#pragma unroll
      for (int it = 0; it < 4; ++it) {
        const u32x2 a = ds_tr(tp + it * 128), cc = ds_tr(tp + it * 128 + 4 * LN_ROW);
        st16(wsr, xo + it * 1024, u32x4{a[0], a[1], cc[0], cc[1]});
      }
    }
    __builtin_amdgcn_sched_barrier(0);
    float t = row_allsum(sw16(s1[0] + s1[1], s2[0] + s2[1]));
    auto sw = __builtin_amdgcn_permlane16_swap(__float_as_uint(t), __float_as_uint(t), false, false);
    const float mean = __uint_as_float(sw[0]) * (1.f / D);
    float var = __uint_as_float(sw[1]) * (1.f / D) - mean * mean;
    if (__builtin_amdgcn_ballot_w64(!(mean * mean <= 16.f * var)) != 0) {
      float qq = 0.f;
#pragma unroll 1
      for (int i = 0; i < 4; ++i) {
        const u32x4 vv = xp[i * 32];
#pragma unroll
        for (int j = 0; j < 4; ++j) { float a = lo16(vv[j]) - mean, b = hi16(vv[j]) - mean; qq += a * a + b * b; }
      }
      qq = row_allsum(qq);
      var = sw16(qq, qq) * (1.f / D);
    }
    const u32 msd = pkbf(mean, __builtin_amdgcn_sqrtf(rbf(rbf(var) + 1e-5f)));
    const float m = lo16(msd), rs = __builtin_amdgcn_rcpf(hi16(msd));
    if (lc < 2) st4(wsr, WS_ST + (lc * 2 + (lr >> 2)) * 4096 + blk * 16 + (lr & 3) * 4, __float_as_uint(lc ? rs : m));
    __builtin_amdgcn_sched_barrier(0);
    float dq[2] = {0.f, 0.f};
#pragma unroll
    for (int i = 0; i < 4; ++i) {
      f32x2 df[4]; u32x4 z;
#pragma unroll
      for (int j = 0; j < 4; ++j) df[j] = f32x2{lo16(v[i][j]), hi16(v[i][j])} - m;
#pragma unroll
      for (int j = 0; j < 4; ++j) z[j] = pkbf(df[j][0], df[j][1]);
#pragma unroll
      for (int j = 0; j < 4; ++j) df[j] = f32x2{lo16(z[j]), hi16(z[j])} * rs;
#pragma unroll
      for (int j = 0; j < 4; ++j) z[j] = pkbf(df[j][0], df[j][1]);
#pragma unroll
      for (int j = 0; j < 4; ++j) dq[j & 1] = dot2(z[j], w[i][j], dq[j & 1]);
    }
    float dt = row_allsum(dq[0] + dq[1]);
    dt = sw16(dt, dt);
    const u32 tag = mktag(__builtin_amdgcn_readfirstlane(gen));
    u32 b = pkbf(dt, dt) & 0xffffu;
    b = (b & 0x8000u) ? (~b & 0xffffu) : (b | 0x8000u);
    u32* rk = (u32*)(smem + MISC0 + 192);
    if (lc == 0) rk[lr] = (b << 16) | (u32)(2047 - (blk * 8 + lr));
    __builtin_amdgcn_sched_barrier(0);
    DRAIN();
    const u32 prev = lds_signal(flg + FL_RK);
    if (prev == 3) {
      const u32 key = wave_umax(rk[lane & 7]);
      lds_wait_ge(flg + FL_INV, 4);
      if (lane < NREP) st8(wsr, WS_F1 + lane * 2048 + blk * 8, u32x2{key, tag});
    } else if (prev == 0) {
      u32x4 f0 = {0u, 0u, 0u, 0u}, f1 = {0u, 0u, 0u, 0u};
      __builtin_amdgcn_s_sleep(40);
#pragma unroll 1
      for (int i = 0; i < SPIN_G; ++i) {
        CBAR();
        if (f0[1] != tag || f0[3] != tag) f0 = ld16(wsr, WS_F1 + rep * 2048 + lane * 16);
        if (f1[1] != tag || f1[3] != tag) f1 = ld16(wsr, WS_F1 + rep * 2048 + 1024 + lane * 16);
        if (__builtin_amdgcn_ballot_w64(f0[1] == tag && f0[3] == tag && f1[1] == tag && f1[3] == tag) == ~0ull) break;
        __builtin_amdgcn_s_sleep(1);
      }
      u32 kmax = umax32(umax32(f0[0], f0[2]), umax32(f1[0], f1[2]));
      kmax = wave_umax(kmax);
      if (lane == 0) lds_st(flg + FL_KEY, kmax);
      asm volatile("s_waitcnt lgkmcnt(0)" ::: "memory");
      if (lane == 0) lds_st(flg + FL_IDX, ((2047u - kmax) & 2047u) + 1u);
    }
    const int idx0 = ((int)lds_wait_ge(flg + FL_IDX, 1) - 1) & (L - 1);
    u32x2 xq; float sm_, sr_;
    if (wv == 0) {
      xq = *(const u32x2*)(x + idx0 * D + (bk & 3) * 256 + lane * 4);
      const u32 so = WS_ST + ((idx0 >> 2) & 1) * 4096 + (idx0 >> 3) * 16 + (idx0 & 3) * 4;
      sm_ = __uint_as_float(ld4(wsr, so)); sr_ = __uint_as_float(ld4(wsr, so + 8192));
    }
    const int stb = tid >> 2, sc = tid & 3;
    u32x4 xr[2]; f32x4 sm[2][4];
#pragma unroll
    for (int k = 0; k < 2; ++k) {
      xr[k] = ld16(wsr, WS_XT + (stb + 128 * k) * 16384 + B * 64 + sc * 16);
#pragma unroll
      for (int j = 0; j < 4; ++j) sm[k][j] = __builtin_bit_cast(f32x4, ld16(wsr, WS_ST + j * 4096 + (stb + 128 * k) * 16));
    }
    if (wv == 0) {
      u32x2 z;
#pragma unroll
      for (int j = 0; j < 2; ++j) {
        const f32x2 df = f32x2{lo16(xq[j]), hi16(xq[j])} - sm_;
        const u32 dd = pkbf(df[0], df[1]);
        const f32x2 zz = f32x2{lo16(dd), hi16(dd)} * sr_;
        z[j] = pkbf(zz[0], zz[1]);
      }
      *(u32x2*)(smem + MISC0 + 2560 + lane * 8) = z;
      if (lane == 0) { lds_st(flg + FL_MSD, __float_as_uint(sm_)); lds_st(flg + FL_MSD + 1, __float_as_uint(sr_)); }
      lds_signal(flg + FL_XV);
    }
    if (tid < 48) *(u32*)(smem + U0 + (tid / 12) * U_STRIDE + (tid % 12) * 4) = 0u;
    char* sdst = smem + U0 + sc * U_STRIDE + (stb + (stb >> 3) + 3) * 16;
#pragma unroll
    for (int k = 0; k < 2; ++k) {
      f32x2 df[4]; u32x4 z;
#pragma unroll
      for (int j = 0; j < 4; ++j) df[j] = f32x2{lo16(xr[k][j]), hi16(xr[k][j])} - f32x2{sm[k][j >> 1][2 * (j & 1)], sm[k][j >> 1][2 * (j & 1) + 1]};
#pragma unroll
      for (int j = 0; j < 4; ++j) z[j] = pkbf(df[j][0], df[j][1]);
#pragma unroll
      for (int j = 0; j < 4; ++j) df[j] = f32x2{lo16(z[j]), hi16(z[j])} * f32x2{sm[k][2 + (j >> 1)][2 * (j & 1)], sm[k][2 + (j >> 1)][2 * (j & 1) + 1]};
#pragma unroll
      for (int j = 0; j < 4; ++j) z[j] = pkbf(df[j][0], df[j][1]);
      *(u32x4*)(sdst + k * 144 * 16) = z;
      *(u32x4*)(sdst + (XR0 - U0) + k * 144 * 16) = xr[k];
    }
    lds_signal(flg + FL_STAGED);
    {
    __builtin_amdgcn_s_setprio(0);
    lds_wait_ge(flg + FL_KG, 4);
    lds_wait_ge(flg + FL_STAGED, 8);
    const char* ub = smem + U0 + c * U_STRIDE;
    const char* kb = smem + KT0 + c * 8192;
      __builtin_amdgcn_s_setprio(2);
      const int a = lane & 31, h = lane >> 5;
      const char* ap0 = kb + (LM - 16 * 33 - a + 8 * h) * 8;
      char* op = smem + YP0 + c * 8192 + a * 128;
#define AFRAG(e) ld2x64(ap0 + (34 - (e)) * 128, 32)
#define BCC(e) (128 - 4 * ((e) >> 1) + (((e) & 1) ? 0 : 2))
#define BFRAG(e) asbf(*(const u32x4*)(bp[(BCC(e) & 7) >> 1] + (BCC(e) >> 3) * 144))
      const char* bp[4];
#pragma unroll
      for (int r = 0; r < 4; ++r) { const int pc = 4 * a + h + 2 * r; bp[r] = ub + (pc + (pc >> 3) + 3) * 16; }
      bf16x8 A2[35], B2[35];
      {
        u32x4 Bq[2];
#pragma unroll
        for (int q = 0; q < 2; ++q) { const int pc = 4 * a + 2 * q + h; Bq[q] = *(const u32x4*)(ub + (pc + (pc >> 3) + 3) * 16); }
        u32 msk = lane == 32 ? 0u : ~0u;
        asm volatile("" : "+v"(msk));
        f32x16 acc = {};
        bf16x8 A[35];
#pragma unroll
        for (int e = 0; e < PFD; ++e) A[e] = AFRAG(e);
#pragma unroll
        for (int e = 0; e < 35; ++e) {
          const int q = 1 - (e & 1);
          if (e + PFD < 35) A[e + PFD] = AFRAG(e + PFD);
          __builtin_amdgcn_sched_barrier(0);
          acc = __builtin_amdgcn_mfma_f32_32x32x16_bf16(A[e], asbf(Bq[q]), acc, 0, 0, 0);
          if (e + 2 < 35) {
#pragma unroll
            for (int j = 0; j < 4; ++j) Bq[q][j] = dppu<DPP_WSHR1>(Bq[q][j]) & msk;
          }
          __builtin_amdgcn_sched_barrier(0);
        }

#pragma unroll
        for (int g = 0; g < 4; ++g) *(f32x4*)(op + ((2 * g + h) ^ (a & 7)) * 16) = f32x4{acc[4 * g], acc[4 * g + 1], acc[4 * g + 2], acc[4 * g + 3]};
        lds_signal(flg + FL_H0);
      }
      u32x2 oh[4];
      final_rows(smem, tid, wv >= 2, fd, idx0, B, wsr, tag, oh, flg + FL_H0);
      __builtin_amdgcn_sched_barrier(0);
      store_row(outr, 4 * tid, idx0, B, oh[0]);
      {
        f32x16 acc = {};
#pragma unroll
        for (int e = 0; e < PFD; ++e) { A2[e] = AFRAG(e); B2[e] = BFRAG(e); }
#pragma unroll
        for (int e = 0; e < 35; ++e) {
          if (e + PFD < 35) { A2[e + PFD] = AFRAG(e + PFD); B2[e + PFD] = BFRAG(e + PFD); }
          __builtin_amdgcn_sched_barrier(0);
          acc = __builtin_amdgcn_mfma_f32_32x32x16_bf16(A2[e], B2[e], acc, 0, 0, 0);
          if (e % 10 == 9) store_row(outr, 4 * tid + 1 + e / 10, idx0, B, oh[1 + e / 10]);
          __builtin_amdgcn_sched_barrier(0);
        }
#pragma unroll
        for (int g = 0; g < 4; ++g) *(f32x4*)(op + 4096 + ((2 * g + h) ^ (a & 7)) * 16) = f32x4{acc[4 * g], acc[4 * g + 1], acc[4 * g + 2], acc[4 * g + 3]};
        lds_signal(flg + FL_PDONE);
      }
    }
    }
  }
}

extern "C" void kernel_launch(void* const* d_in, const int* in_sizes, int n_in,
                              void* d_out, int out_size, void* d_ws, size_t ws_size,
                              hipStream_t stream) {
  fused<<<256, 768, 0, stream>>>((const u16*)d_in[0], (const u16*)d_in[8], (const u16*)d_in[1], (const u16*)d_in[2],
                                 (const u16*)d_in[3], (const u16*)d_in[4], (const u16*)d_in[5], (const u16*)d_in[6],
                                 (const u16*)d_in[9], (const u16*)d_in[10], (const u16*)d_in[11], (char*)d_ws, (u16*)d_out);
}
```
